```python
import torch

def _softmax(x, axis=-1):
    return torch.softmax(x, dim=axis)
def _gelu(x):
    return torch.nn.functional.gelu(x, approximate="tanh")
def _silu(x):
    return torch.nn.functional.silu(x)
def _sigmoid(x):
    return torch.sigmoid(x)
def _layernorm(x, axis=-1, eps=1e-5):
    m = x.mean(dim=axis, keepdim=True)
    v = x.var(dim=axis, keepdim=True, unbiased=False)
    return (x - m) / torch.sqrt(v + eps)
def _scatter_add(tgt, idx, src, axis=0):
    return tgt.clone().index_add_(axis, idx, src)
def _segment_sum(src, seg, n):
    out = torch.zeros((n,) + src.shape[1:], dtype=src.dtype)
    return out.index_add_(0, seg, src)
def _pad(x, pw):
    flat = tuple(v for pair in reversed(pw) for v in pair)
    return torch.nn.functional.pad(x, flat)

kernel_name = 'gated_mix__linear_kernel_hedgehog__softmax_full_mha__prompt'

def setup_inputs():

    _w = (lambda a: (lambda t: t.to(torch.bfloat16) if t.is_floating_point() else t)(torch.as_tensor(a)))
    def _A_setup():
        B, S, D, H = 1, 2048, 640, 32
        Dh = D // H
        return {
            "x": _w((lambda *s: torch.randn(*s, dtype=torch.bfloat16) * 0.05)(B, S, D)),
            "wqkv": _w((lambda *s: torch.randn(*s, dtype=torch.bfloat16) * 1.6)(D, 3 * D)),
            "w_phi": _w((lambda *s: torch.randn(*s, dtype=torch.bfloat16) * 1.6)(H, Dh, Dh) * (Dh ** -0.5)),
            "wo": _w((lambda *s: torch.randn(*s, dtype=torch.bfloat16) * 0.05)(D, D)),
        }
    _A_in = _A_setup()

    def _B_setup():
        B, S, D = 1, 2048, 640
        return {
            "x": _w((lambda *s: torch.randn(*s, dtype=torch.bfloat16) * 0.05)(B, S, D)),
            "wqkv": _w((lambda *s: torch.randn(*s, dtype=torch.bfloat16) * 0.8)(D, 3 * D)),
            "wo": _w((lambda *s: torch.randn(*s, dtype=torch.bfloat16) * 0.05)(D, D)),
        }
    _B_in = _B_setup()

    return {
        "A_x": _A_in["x"],
        "A_wqkv": _A_in["wqkv"],
        "A_w_phi": _A_in["w_phi"],
        "A_wo": _A_in["wo"],
        "B_x": _B_in["x"],
        "B_wqkv": _B_in["wqkv"],
        "B_wo": _B_in["wo"],
        "gate_w": _w((lambda *s: torch.randn(*s, dtype=torch.bfloat16) * 0.05)(640) * 16.0),
    }

def reference(A_x, A_wqkv, A_w_phi, A_wo, B_x, B_wqkv, B_wo, gate_w):
    def _A_ref(x, wqkv, w_phi, wo):
        B, S, D = x.shape
        H = 32; Dh = D // H
        qkv = (lambda x, *d: x.permute(*d))((x @ wqkv).reshape(B, S, 3, H, Dh), 2, 0, 3, 1, 4)
        q, k, v = qkv[0], qkv[1], qkv[2]
        pq = torch.einsum("bhsd,hdf->bhsf", q, w_phi)
        pk = torch.einsum("bhsd,hdf->bhsf", k, w_phi)
        phi_q = torch.concatenate([_softmax(pq, axis=-1), _softmax(-pq, axis=-1)], axis=-1)
        phi_k = torch.concatenate([_softmax(pk, axis=-1), _softmax(-pk, axis=-1)], axis=-1)
        kv = torch.einsum("bhsf,bhsd->bhfd", phi_k, v)
        z  = torch.einsum("bhsf->bhf", phi_k)
        num = torch.einsum("bhqf,bhfd->bhqd", phi_q, kv)
        den = torch.einsum("bhqf,bhf->bhq", phi_q, z)[..., None] + 1e-6
        o = num / den
        return (lambda x, *d: x.permute(*d))(o, 0, 2, 1, 3).reshape(B, S, D) @ wo

    def _B_ref(x, wqkv, wo):
        B, S, D = x.shape
        H = 32; Dh = D // H
        qkv = (lambda x, *d: x.permute(*d))((x @ wqkv).reshape(B, S, 3, H, Dh), 2, 0, 3, 1, 4)
        q, k, v = qkv[0], qkv[1], qkv[2]
        scale = Dh ** -0.5
        scores = torch.einsum("bhqd,bhkd->bhqk", q, k) * scale
        p = _softmax(scores, axis=-1)
        o = torch.einsum("bhqk,bhkd->bhqd", p, v)
        return (lambda x, *d: x.permute(*d))(o, 0, 2, 1, 3).reshape(B, S, D) @ wo

    x = A_x
    g = _sigmoid((x * gate_w).sum(-1, keepdims=True))
    a = _A_ref(x, A_wqkv, A_w_phi, A_wo)
    b = _B_ref(x, B_wqkv, B_wo)
    return g * a + (1 - g) * b
```

```cpp
#include <hip/hip_runtime.h>
#include <hip/hip_bf16.h>
#include <stdint.h>

typedef short s16x8 __attribute__((ext_vector_type(8)));
typedef short s16x4 __attribute__((ext_vector_type(4)));
typedef float f32x2 __attribute__((ext_vector_type(2)));
typedef float f32x4 __attribute__((ext_vector_type(4)));
typedef float f32x16 __attribute__((ext_vector_type(16)));
typedef float f32x32 __attribute__((ext_vector_type(32)));
typedef unsigned u32x2 __attribute__((ext_vector_type(2)));
typedef unsigned u32x4 __attribute__((ext_vector_type(4)));
typedef __bf16 bf16x2 __attribute__((ext_vector_type(2)));
#define LDSP __attribute__((address_space(3)))
#define GLBP __attribute__((address_space(1)))
#define DI __device__ __forceinline__
#define SBAR() __builtin_amdgcn_sched_barrier(0)
#define PIN(x) asm volatile("" : "+v"(x))
#define MFMA16(a, b, c) __builtin_amdgcn_mfma_f32_32x32x16_bf16((a), (b), (c), 0, 0, 0)
#define MFMA8(a, b, c) __builtin_amdgcn_mfma_f32_32x32x8bf16_1k((a), (b), (c), 0, 0, 0)
#define MFMA4B(a, b, c) __builtin_amdgcn_mfma_f32_32x32x4bf16_1k((a), (b), (c), 0, 0, 0)
#define MFMAS(a, b, c) __builtin_amdgcn_mfma_f32_16x16x32_bf16((a), (b), (c), 0, 0, 0)

constexpr unsigned QKB = 2560;
constexpr unsigned VBK = 2688;
constexpr size_t SEC_QK = 32ull * 32 * QKB, SEC_V = 32ull * 32 * VBK;
constexpr size_t OFF_QA = 0, OFF_KA = SEC_QK, OFF_QB = 2 * SEC_QK, OFF_KB = 3 * SEC_QK;
constexpr size_t OFF_VA = 4 * SEC_QK, OFF_VB = OFF_VA + SEC_V;
constexpr size_t OFF_O = OFF_VB + SEC_V;
constexpr size_t OFF_WOF = OFF_O + 2048ull * 1280 * 2;
constexpr size_t OFF_G = OFF_WOF + 1280ull * 640 * 2;
constexpr size_t OFF_KVP = OFF_G + 2048 * 8;
constexpr size_t OFF_CNT = OFF_KVP + 32ull * 8 * 5120;
constexpr float LOG2E = 1.4426950408889634f;
constexpr float QSCALE = 0.22360679774997896f * 1.4426950408889634f;

DI unsigned cvtpk(float a, float b) { f32x2 v = {a, b}; return __builtin_bit_cast(unsigned, __builtin_convertvector(v, bf16x2)); }
DI float bfr(float a) { return __uint_as_float(cvtpk(a, a) & 0xffff0000u); }
DI float bflo(unsigned u) { return __uint_as_float(u << 16); }
DI float bfhi(unsigned u) { return __uint_as_float(u & 0xffff0000u); }
DI float ex2(float x) { return __builtin_amdgcn_exp2f(x); }
DI float swap32(float v, int hh) {
    auto r = __builtin_amdgcn_permlane32_swap(__float_as_uint(v), __float_as_uint(v), false, false);
    return __uint_as_float(hh ? r[0] : r[1]);
}
DI void glds16(unsigned voff, const void* sbase, unsigned ldsaddr) {
    unsigned keep;
    asm volatile("s_mov_b32 %0, m0\n\ts_mov_b32 m0, %3\n\ts_nop 0\n\tglobal_load_lds_dwordx4 %1, %2\n\ts_mov_b32 m0, %0"
                 : "=&s"(keep) : "v"(voff), "s"(sbase), "s"(ldsaddr) : "memory");
}
#define WAITV(n) asm volatile("s_waitcnt vmcnt(" #n ")" ::: "memory")
#define WAITV_BAR(n) asm volatile("s_waitcnt vmcnt(" #n ") lgkmcnt(0)\n\ts_barrier" ::: "memory")
#define GLD4(dst, voff, sb) asm volatile("global_load_dwordx4 %0, %1, %2" : "=v"(dst) : "v"(voff), "s"(sb) : "memory")
#define GLD2(dst, voff, sb) asm volatile("global_load_dwordx2 %0, %1, %2" : "=v"(dst) : "v"(voff), "s"(sb) : "memory")
#define GLDU(dst, voff, sb, imm) asm volatile("global_load_ushort %0, %1, %2 offset:" #imm : "=v"(dst) : "v"(voff), "s"(sb) : "memory")
typedef short v4i16_t __attribute__((ext_vector_type(4)));
DI s16x4 trread(const LDSP char* p) { return __builtin_bit_cast(s16x4, __builtin_amdgcn_ds_read_tr16_b64_v4i16((LDSP v4i16_t*)p)); }
DI s16x8 cat8(s16x4 a, s16x4 b) { return __builtin_shufflevector(a, b, 0, 1, 2, 3, 4, 5, 6, 7); }
DI s16x8 ld8(const LDSP char* p) { return *(const LDSP s16x8*)p; }
DI s16x4 ld4(const LDSP char* p) { return *(const LDSP s16x4*)p; }

constexpr unsigned P1_STAGE = 49152;

template <bool VT>
DI void p1_tile(const char* x, const char* w, char* dst, int mt, int colbase, float scale, LDSP char* lds) {
    const int tid = threadIdx.x, lane = tid & 63, r = lane & 31, hh = lane >> 5;
    const int wid = __builtin_amdgcn_readfirstlane(tid >> 6), wt = wid >> 1, wc = wid & 1;
    const unsigned lds0 = (unsigned)(uintptr_t)lds;
    unsigned xo[4], wo[2];
#pragma unroll
    for (int i = 0; i < 4; ++i) { const int tok = 32 * wid + 8 * i + (lane >> 3); xo[i] = tok * 1280 + (((lane & 7) ^ ((tok >> 1) & 7)) << 4); }
#pragma unroll
    for (int i = 0; i < 2; ++i) wo[i] = (4 * (2 * wid + i) + ((lane >> 2) & 3)) * 3840 + (lane >> 4) * 64 + (lane & 3) * 16;
    const char* xs = x + (size_t)mt * 256 * 1280;
    auto issue = [&](int s, unsigned bufoff) {
        const char* xb = xs + s * 128; const char* wb = w + (size_t)s * (64 * 3840);
#pragma unroll
        for (int i = 0; i < 4; ++i) glds16(xo[i], xb, lds0 + bufoff + (4 * wid + i) * 1024);
#pragma unroll
        for (int i = 0; i < 2; ++i) glds16(wo[i], wb, lds0 + bufoff + 32768 + (2 * wid + i) * 1024);
    };
    issue(0, 0); issue(1, P1_STAGE); issue(2, 2 * P1_STAGE);
    unsigned xr[4];
    { const int tok = 64 * wt + r; const unsigned f = (unsigned)(hh ^ ((tok >> 1) & 7)) << 4;
#pragma unroll
      for (int ks = 0; ks < 4; ++ks) xr[ks] = tok * 128 + (f ^ (ks << 5)); }
    const unsigned wr = 32768 + (2 * hh) * 1024 + (2 * wc) * 256 + ((lane & 15) >> 2) * 64 + ((lane >> 4) & 1) * 32 + (lane & 3) * 8;
    f32x16 acc[2][2];
#pragma unroll
    for (int i = 0; i < 2; ++i)
#pragma unroll
        for (int j = 0; j < 2; ++j) acc[i][j] = f32x16{};
    unsigned cur = 0, nxt = 2 * P1_STAGE;
    for (int s = 0; s < 10; ++s) {
        if (s == 0) WAITV_BAR(12); else if (s < 9) WAITV_BAR(6); else WAITV_BAR(0);
        if (s >= 1 && s <= 7) issue(s + 2, nxt);
        const LDSP char* b = lds + cur;
#pragma unroll
        for (int ks = 0; ks < 4; ++ks) {
            s16x8 xf[2], wf[2];
#pragma unroll
            for (int t = 0; t < 2; ++t) xf[t] = ld8(b + xr[ks] + t * 4096);
#pragma unroll
            for (int c = 0; c < 2; ++c) wf[c] = cat8(trread(b + wr + ks * 4096 + c * 256), trread(b + wr + ks * 4096 + c * 256 + 1024));
#pragma unroll
            for (int t = 0; t < 2; ++t)
#pragma unroll
                for (int c = 0; c < 2; ++c) acc[t][c] = VT ? MFMA16(xf[t], wf[c], acc[t][c]) : MFMA16(wf[c], xf[t], acc[t][c]);
        }
        nxt = cur; cur = (cur == 2 * P1_STAGE) ? 0 : cur + P1_STAGE;
    }
    const int tp = mt * 4 + wt;
    if (!VT) {
#pragma unroll
        for (int c = 0; c < 2; ++c)
#pragma unroll
            for (int qd = 0; qd < 4; ++qd) {
                const int G = (colbase + 64 * wc + 32 * c + 8 * qd + 4 * hh) >> 2, h = (G * 205) >> 10, g = G - 5 * h;
                const unsigned o0 = (h * 32 + tp) * QKB + (g < 4 ? (g >> 1) * 512 + r * 16 + (g & 1) * 8 : 2048 + r * 8);
                const unsigned ts = g < 4 ? 1024 : 256;
#pragma unroll
                for (int t = 0; t < 2; ++t) {
                    u32x2 v = {cvtpk(acc[t][c][4 * qd] * scale, acc[t][c][4 * qd + 1] * scale), cvtpk(acc[t][c][4 * qd + 2] * scale, acc[t][c][4 * qd + 3] * scale)};
                    *(u32x2*)(dst + o0 + t * ts) = v;
                }
            }
    } else {
#pragma unroll
        for (int c = 0; c < 2; ++c) {
            const int col = colbase + 64 * wc + 32 * c + r, h = (col * 3277) >> 16, d = col - 20 * h;
            const unsigned o0 = (h * 32 + tp) * VBK + hh * 336 + d * 16;
#pragma unroll
            for (int t = 0; t < 2; ++t)
#pragma unroll
                for (int s = 0; s < 2; ++s) {
                    u32x4 v = {cvtpk(acc[t][c][8 * s], acc[t][c][8 * s + 1]), cvtpk(acc[t][c][8 * s + 2], acc[t][c][8 * s + 3]),
                               cvtpk(acc[t][c][8 * s + 4], acc[t][c][8 * s + 5]), cvtpk(acc[t][c][8 * s + 6], acc[t][c][8 * s + 7])};
                    *(u32x4*)(dst + o0 + t * 1344 + s * 672) = v;
                    if (d == 19) *(u32x4*)(dst + o0 + t * 1344 + s * 672 + 16) = u32x4{0x3f803f80u, 0x3f803f80u, 0x3f803f80u, 0x3f803f80u};
                }
        }
    }
}

DI void p1_spare(const char* x, const char* gate_w, const unsigned short* woA, const unsigned short* woB, char* ws, int sp) {
    const int tid = threadIdx.x, lane = tid & 63, wid = tid >> 6;
    if (sp == 0 && tid < 32) *(unsigned*)(ws + OFF_CNT + tid * 256) = 0u;
    u32x4 gw[5];
#pragma unroll
    for (int i = 0; i < 5; ++i) gw[i] = *(const u32x4*)(gate_w + ((lane & 15) + 16 * i) * 16);
#pragma unroll
    for (int it = 0; it < 4; ++it) {
        const int t = sp * 128 + wid * 16 + it * 4 + (lane >> 4);
        float acc = 0.f;
#pragma unroll
        for (int i = 0; i < 5; ++i) {
            const u32x4 xv = *(const u32x4*)(x + (size_t)t * 1280 + ((lane & 15) + 16 * i) * 16);
#pragma unroll
            for (int j = 0; j < 4; ++j) acc += bfr(bflo(xv[j]) * bflo(gw[i][j])) + bfr(bfhi(xv[j]) * bfhi(gw[i][j]));
        }
#pragma unroll
        for (int m = 1; m < 16; m <<= 1) acc += __shfl_xor(acc, m, 64);
        const float lg = bfr(acc), g = bfr(1.f / (1.f + __expf(-lg))), g1 = bfr(1.f - g);
        if ((lane & 15) == 0) *(f32x2*)(ws + OFF_G + t * 8) = f32x2{g, g1};
    }
    for (int idx = tid; idx < 6400; idx += 512) {
        const int cid = sp * 6400 + idx, l = cid & 63, fk = cid >> 6, nt = fk / 40, ks = fk - nt * 40;
        const int ch = ks * 4 + (l >> 4), h = ch / 5, i = ch - 5 * h, n = nt * 16 + (l & 15);
        const unsigned short* p0; const unsigned short* p1;
        if (i < 4) { const unsigned short* wsel = (i < 2) ? woA : woB; p0 = wsel + (h * 20 + 4 * (i & 1)) * 640 + n; p1 = p0 + 8 * 640; }
        else { p0 = woA + (h * 20 + 16) * 640 + n; p1 = woB + (h * 20 + 16) * 640 + n; }
        u32x4 v = {(unsigned)p0[0] | ((unsigned)p0[640] << 16), (unsigned)p0[1280] | ((unsigned)p0[1920] << 16),
                   (unsigned)p1[0] | ((unsigned)p1[640] << 16), (unsigned)p1[1280] | ((unsigned)p1[1920] << 16)};
        *(u32x4*)(ws + OFF_WOF + (size_t)cid * 16) = v;
    }
}

__global__ __launch_bounds__(512, 2) void p1_kernel(const char* x, const char* wA, const char* wB, const char* gate_w,
                                                    const unsigned short* woA, const unsigned short* woB, char* ws) {
    __shared__ __attribute__((aligned(16))) char smem[3 * P1_STAGE];
    const int b = blockIdx.x, xcd = b & 7, slot = b >> 3, a = xcd & 1, bq = xcd >> 1;
    const int ntl = slot >> 2, mt = 4 * a + (slot & 3);
    if (bq >= 2 && ntl == 7) { p1_spare(x, gate_w, woA, woB, ws, (bq - 2) * 8 + a * 4 + (slot & 3)); return; }
    const int nt = (bq == 3 ? 23 : 8 * bq) + ntl;
    const int sec = nt / 5, colbase = (nt - 5 * sec) * 128;
    const int br = sec >= 3, sm = sec - 3 * br;
    const char* w = (br ? wB : wA) + (sm * 640 + colbase) * 2;
    if (sm == 2) p1_tile<true>(x, w, ws + (br ? OFF_VB : OFF_VA), mt, colbase, 1.f, (LDSP char*)smem);
    else p1_tile<false>(x, w, ws + (size_t)(2 * br + sm) * SEC_QK, mt, colbase, (sec == 3) ? QSCALE : 1.f, (LDSP char*)smem);
}

constexpr unsigned P2_BUF = 41984, P2_V = 20480;
constexpr unsigned P2_TR = 3 * P2_BUF, P2_KVF = P2_TR + 8 * 2560, P2_FLAG = P2_KVF + 2560, P2_LDS = P2_FLAG + 64;

DI void features(s16x8 f1, s16x4 f2, s16x8 w1, s16x4 w2, int hh, unsigned (&pk)[10]) {
    f32x16 p = MFMA16(w1, f1, f32x16{});
    p = MFMA8(w2, f2, p);
    float v[10];
#pragma unroll
    for (int i = 0; i < 10; i += 2) { const unsigned u = cvtpk(p[i], p[i + 1]); v[i] = bflo(u); v[i + 1] = bfhi(u); }
    float mx = v[0], mn = v[0];
#pragma unroll
    for (int i = 1; i < 10; ++i) { mx = fmaxf(mx, v[i]); mn = fminf(mn, v[i]); }
    mx = fmaxf(mx, swap32(mx, hh)); mn = fminf(mn, swap32(mn, hh));
    const float a = -mx * LOG2E, b = mn * LOG2E;
    float ep[10], en[10], sp = 0.f, sn = 0.f;
#pragma unroll
    for (int i = 0; i < 10; ++i) { ep[i] = ex2(fmaf(v[i], LOG2E, a)); en[i] = ex2(fmaf(v[i], -LOG2E, b)); sp += ep[i]; sn += en[i]; }
    sp += swap32(sp, hh); sn += swap32(sn, hh);
    const float rp = __builtin_amdgcn_rcpf(sp), rn = __builtin_amdgcn_rcpf(sn);
#pragma unroll
    for (int i = 0; i < 5; ++i) { pk[i] = cvtpk(ep[2 * i] * rp, ep[2 * i + 1] * rp); pk[5 + i] = cvtpk(en[2 * i] * rn, en[2 * i + 1] * rn); }
}
DI f32x32 cat32(f32x16 a, f32x16 b) {
    return __builtin_shufflevector(a, b, 0, 1, 2, 3, 4, 5, 6, 7, 8, 9, 10, 11, 12, 13, 14, 15, 16, 17, 18, 19, 20, 21, 22, 23, 24, 25, 26, 27, 28, 29, 30, 31);
}
DI float rowmax32(const f32x32& s, float m) {
#pragma unroll
    for (int i = 0; i < 32; i += 2) m = fmaxf(fmaxf(m, s[i]), s[i + 1]);
    return m;
}

__global__ __launch_bounds__(512, 2) void p2_kernel(const char* wphi, char* ws) {
    __shared__ __attribute__((aligned(16))) char smem[P2_LDS];
    LDSP char* lds = (LDSP char*)smem;
    const unsigned lds0 = (unsigned)(uintptr_t)lds;
    const int tid = threadIdx.x, lane = tid & 63, r = lane & 31, hh = lane >> 5;
    const int wid = __builtin_amdgcn_readfirstlane(tid >> 6);
    const int b = blockIdx.x, h = (b & 7) * 4 + ((b >> 3) & 3), qb = b >> 5;
    const int q0 = qb * 256 + wid * 32, tp = q0 >> 6, e = wid & 1;

    s16x8 Q1, qA1, kA1, vA0, vA1; s16x4 Q2, qA2, kA2; f32x2 gg;
    unsigned wv[12];
    {
        const char* qk = ws + (size_t)(h * 32 + tp) * QKB;
        const unsigned o1 = e * 1024 + lane * 16, o2 = 2048 + (e * 32 + r) * 8;
        GLD4(kA1, o1, qk + OFF_KA); GLD2(kA2, o2, qk + OFF_KA);
        const int f = r < 18 ? r : (r == 20 || r == 21) ? r - 2 : 0;
        const char* wp = wphi + h * 800;
        const unsigned ow = hh * 320 + f * 2, ow2 = 640 + f * 2;
        GLDU(wv[0], ow, wp, 0);   GLDU(wv[1], ow, wp, 40);  GLDU(wv[2], ow, wp, 80);   GLDU(wv[3], ow, wp, 120);
        GLDU(wv[4], ow, wp, 160); GLDU(wv[5], ow, wp, 200); GLDU(wv[6], ow, wp, 240);  GLDU(wv[7], ow, wp, 280);
        GLDU(wv[8], ow2, wp, 0);  GLDU(wv[9], ow2, wp, 40); GLDU(wv[10], ow2, wp, 80); GLDU(wv[11], ow2, wp, 120);
        const char* va = ws + OFF_VA + (size_t)(h * 32 + tp) * VBK;
        const unsigned ov = e * 1344 + hh * 336 + (r < 20 ? r : 20) * 16, ov1 = ov + 672;
        GLD4(vA0, ov, va); GLD4(vA1, ov1, va);
        GLD4(qA1, o1, qk + OFF_QA); GLD2(qA2, o2, qk + OFF_QA);
        GLD4(Q1, o1, qk + OFF_QB); GLD2(Q2, o2, qk + OFF_QB);
        const unsigned og = (q0 + r) * 8;
        GLD2(gg, og, ws + OFF_G);
    }
    const char* gK = ws + OFF_KB + (size_t)h * 32 * QKB;
    const char* gV = ws + OFF_VB + (size_t)h * 32 * VBK;
    const unsigned l16 = lane * 16;
    auto issue = [&](int st, unsigned buf) {
#pragma unroll
        for (int i = 0; i < 6; ++i) {
            const int p = i < 5 ? wid + 8 * i : 40;
            const char* src = p < 20 ? gK + st * 20480 + p * 1024 : gV + st * 21504 + (p - 20) * 1024;
            if (i < 5 || wid == 0) glds16(l16, src, lds0 + buf + p * 1024);
        }
    };
    issue(0, 0); issue(1, P2_BUF);
    asm volatile("s_waitcnt vmcnt(10)" : "+v"(Q1), "+v"(qA1), "+v"(kA1), "+v"(vA0), "+v"(vA1), "+v"(Q2), "+v"(qA2), "+v"(kA2), "+v"(gg),
                 "+v"(wv[0]), "+v"(wv[1]), "+v"(wv[2]), "+v"(wv[3]), "+v"(wv[4]), "+v"(wv[5]), "+v"(wv[6]), "+v"(wv[7]),
                 "+v"(wv[8]), "+v"(wv[9]), "+v"(wv[10]), "+v"(wv[11]) :: "memory");
    unsigned phq[10];
    {
        const s16x8 wf1 = __builtin_bit_cast(s16x8, u32x4{wv[0] | (wv[1] << 16), wv[2] | (wv[3] << 16), wv[4] | (wv[5] << 16), wv[6] | (wv[7] << 16)});
        const s16x4 wf2 = __builtin_bit_cast(s16x4, u32x2{hh ? 0u : wv[8] | (wv[9] << 16), hh ? 0u : wv[10] | (wv[11] << 16)});
        unsigned phk[10];
        features(kA1, kA2, wf1, wf2, hh, phk);
        LDSP char* img = lds + P2_TR + wid * 2560;
#pragma unroll
        for (int i = 0; i < 5; ++i) *(LDSP u32x2*)(img + r * 80 + 16 * i + 8 * hh) = u32x2{phk[2 * i], phk[2 * i + 1]};
        features(qA1, qA2, wf1, wf2, hh, phq);
        const int tq = (lane & 15) >> 2, p = lane & 3, blk = (lane >> 4) & 1;
        const LDSP char* t0 = img + (4 * hh + tq) * 80 + blk * 32 + p * 8;
        const LDSP char* t1 = img + (4 * hh + tq) * 80 + 64 + (p & 1) * 8;
        f32x16 d0 = MFMA16(cat8(trread(t0), trread(t0 + 640)), vA0, f32x16{});
        f32x16 d1 = MFMA16(cat8(trread(t1), trread(t1 + 640)), vA0, f32x16{});
        d0 = MFMA16(cat8(trread(t0 + 1280), trread(t0 + 1920)), vA1, d0);
        d1 = MFMA16(cat8(trread(t1 + 1280), trread(t1 + 1920)), vA1, d1);
        LDSP char* sc = lds + 2 * P2_BUF + wid * 5120 + l16;
#pragma unroll
        for (int i = 0; i < 4; ++i) *(LDSP f32x4*)(sc + i * 1024) = f32x4{d0[4 * i], d0[4 * i + 1], d0[4 * i + 2], d0[4 * i + 3]};
        *(LDSP f32x4*)(sc + 4096) = f32x4{d1[0], d1[1], d1[2], d1[3]};
    }
    if (tid == 0) *(LDSP unsigned*)(lds + P2_FLAG) = 0u;
    if (wid == 0) WAITV_BAR(6); else WAITV_BAR(5);
    char* kvp = ws + OFF_KVP + (size_t)h * 8 * 5120;
    GLBP unsigned* cnt = (GLBP unsigned*)(ws + OFF_CNT + h * 256);
    if (wid < 5) {
        const LDSP char* sc = lds + 2 * P2_BUF + wid * 1024 + l16;
        f32x4 s = *(const LDSP f32x4*)sc;
#pragma unroll
        for (int i = 1; i < 8; ++i) s += *(const LDSP f32x4*)(sc + i * 5120);
        const char* dstp = kvp + qb * 5120 + wid * 1024;
        asm volatile("global_store_dwordx4 %0, %1, %2 sc1\n\ts_nop 1" :: "v"(l16), "v"(s), "s"(dstp) : "memory");
    }

    const unsigned ka = l16, k2a = 2048 + lane * 8, vaa = P2_V + hh * 336 + (r < 20 ? r : 20) * 16;
    f32x16 O;
    float lsum;
    for (int pass = 0;; ++pass) {
        float m;
        if (pass) {
            m = -3.0e38f;
            for (int st = 0; st < 4; ++st) {
                WAITV_BAR(0);
                issue(st, 0);
                WAITV_BAR(0);
                for (int j = 0; j < 8; ++j) {
                    const LDSP char* kb = lds + j * QKB;
                    f32x32 s = MFMA4B(ld4(kb + k2a), Q2, cat32(MFMA16(ld8(kb + ka), Q1, f32x16{}), MFMA16(ld8(kb + ka + 1024), Q1, f32x16{})));
                    m = rowmax32(s, m);
                }
            }
            WAITV_BAR(0);
            issue(0, 0); issue(1, P2_BUF);
            WAITV_BAR(0);
        }
        const LDSP char* kp = lds + ka;
        const LDSP char* k2p = lds + k2a;
        const LDSP char* vp = lds + vaa;
        s16x8 Ka = ld8(kp), Kb = ld8(kp + 1024); s16x4 K2 = ld4(k2p);
        if (!pass) {
            f32x32 s = MFMA4B(K2, Q2, cat32(MFMA16(Ka, Q1, f32x16{}), MFMA16(Kb, Q1, f32x16{})));
            m = rowmax32(s, -3.0e38f);
        }
        m = fmaxf(m, swap32(m, hh));
        f32x16 negm;
#pragma unroll
        for (int i = 0; i < 16; ++i) negm[i] = -m;
        PIN(negm);
        f32x32 SA = MFMA4B(K2, Q2, cat32(MFMA16(Ka, Q1, negm), MFMA16(Kb, Q1, negm))), SB;
        Ka = ld8(kp + QKB); Kb = ld8(kp + QKB + 1024); K2 = ld4(k2p + QKB);
        s16x8 V0 = ld8(vp), V1 = ld8(vp + 672), V2 = ld8(vp + 1344), V3 = ld8(vp + 2016);
        O = f32x16{};
        u32x4 pw;
#define EXP2(S, i) do { S[i] = ex2(S[i]); S[(i) + 1] = ex2(S[(i) + 1]); } while (0)
#define PK4(S, i) u32x4{cvtpk(S[i], S[(i) + 1]), cvtpk(S[(i) + 2], S[(i) + 3]), cvtpk(S[(i) + 4], S[(i) + 5]), cvtpk(S[(i) + 6], S[(i) + 7])}
#define PF __builtin_bit_cast(s16x8, pw)
#define STEP(C, N, QK, LK, LV, ko, vo) do { \
        f32x16 na, nb; \
        SBAR(); \
        if (QK) na = MFMA16(Ka, Q1, negm); \
        EXP2(C, 0); EXP2(C, 2); EXP2(C, 4); PIN(C); SBAR(); \
        if (QK) nb = MFMA16(Kb, Q1, negm); \
        EXP2(C, 6); pw = PK4(C, 0); EXP2(C, 8); PIN(pw); PIN(C); SBAR(); \
        O = MFMA16(V0, PF, O); \
        if (LK) { Ka = ld8(kp + (ko)); Kb = ld8(kp + (ko) + 1024); } \
        EXP2(C, 10); EXP2(C, 12); EXP2(C, 14); PIN(C); SBAR(); \
        if (QK) N = MFMA4B(K2, Q2, cat32(na, nb)); \
        if (LV) V0 = ld8(vp + (vo)); \
        if (LK) K2 = ld4(k2p + (ko)); \
        pw = PK4(C, 8); EXP2(C, 16); EXP2(C, 18); EXP2(C, 20); PIN(pw); PIN(C); SBAR(); \
        O = MFMA16(V1, PF, O); \
        if (LV) V1 = ld8(vp + (vo) + 672); \
        EXP2(C, 22); pw = PK4(C, 16); EXP2(C, 24); PIN(pw); PIN(C); SBAR(); \
        O = MFMA16(V2, PF, O); \
        if (LV) V2 = ld8(vp + (vo) + 1344); \
        EXP2(C, 26); EXP2(C, 28); EXP2(C, 30); pw = PK4(C, 24); PIN(pw); SBAR(); \
        O = MFMA16(V3, PF, O); \
        if (LV) V3 = ld8(vp + (vo) + 2016); \
        SBAR(); } while (0)
        f32x4 part[8];
        for (int n = 0; n < 4; ++n) {
            if (n) {
                if (!pass && n == 2) {
                    if (wid == 0) {
                        while (__builtin_amdgcn_readfirstlane(__hip_atomic_load(cnt, __ATOMIC_RELAXED, __HIP_MEMORY_SCOPE_AGENT)) < 40u) __builtin_amdgcn_s_sleep(2);
                    }
                }
                if (!pass && n == 3 && wid < 5) {
                    const f32x4 s = ((part[0] + part[1]) + (part[2] + part[3])) + ((part[4] + part[5]) + (part[6] + part[7]));
                    *(LDSP u32x2*)(lds + P2_KVF + wid * 512 + lane * 8) = u32x2{cvtpk(s[0], s[1]), cvtpk(s[2], s[3])};
                }
                WAITV_BAR(0);
                if (!pass && n == 1 && wid < 5 && lane == 0) __hip_atomic_fetch_add(cnt, 1u, __ATOMIC_RELAXED, __HIP_MEMORY_SCOPE_AGENT);
                if (n == 1) issue(2, 2 * P2_BUF);
                if (n == 2) {
                    issue(3, 0);
                    if (!pass && wid < 5) {
                        const auto rs = __builtin_amdgcn_make_buffer_rsrc(kvp, 0, 8 * 5120, 0x00020000);
#pragma unroll
                        for (int i = 0; i < 8; ++i) part[i] = __builtin_bit_cast(f32x4, __builtin_amdgcn_raw_buffer_load_b128(rs, l16, wid * 1024 + i * 5120, 16));
                    }
                }
                const unsigned nb_ = (n == 3 ? 0 : n) * P2_BUF;
                kp = lds + nb_ + ka; k2p = lds + nb_ + k2a;
                STEP(SA, SB, true, true, true, 0, 7 * VBK);
                vp = lds + nb_ + vaa;
                STEP(SB, SA, true, true, true, QKB, 0);
            }
            STEP(SA, SB, true, true, true, 2 * QKB, 1 * VBK);
            STEP(SB, SA, true, true, true, 3 * QKB, 2 * VBK);
            STEP(SA, SB, true, true, true, 4 * QKB, 3 * VBK);
            STEP(SB, SA, true, true, true, 5 * QKB, 4 * VBK);
            STEP(SA, SB, true, true, true, 6 * QKB, 5 * VBK);
            STEP(SB, SA, true, true, true, 7 * QKB, 6 * VBK);
        }
        STEP(SA, SB, true, false, true, 0, 7 * VBK);
        STEP(SB, SA, false, false, false, 0, 0);
        { const float o8 = O[8], p8 = swap32(o8, hh); lsum = hh ? o8 : p8; }
        if (pass) break;
        float chk = lsum;
#pragma unroll
        for (int i = 0; i < 8; ++i) chk += fabsf(O[i]);
        if (!hh) chk += (fabsf(O[8]) + fabsf(O[9])) + (fabsf(O[10]) + fabsf(O[11]));
        if (!(chk < 1.0e37f)) *(LDSP unsigned*)(lds + P2_FLAG) = 1u;
        WAITV_BAR(0);
        if (__builtin_amdgcn_readfirstlane(*(LDSP unsigned*)(lds + P2_FLAG)) == 0u) break;
    }

    const LDSP char* kf = lds + P2_KVF + lane * 8;
    f32x16 nm = MFMA16(cat8(ld4(kf), ld4(kf + 512)), __builtin_bit_cast(s16x8, u32x4{phq[0], phq[1], phq[2], phq[3]}), f32x16{});
    nm = MFMA16(cat8(ld4(kf + 1024), ld4(kf + 1536)), __builtin_bit_cast(s16x8, u32x4{phq[4], phq[5], phq[6], phq[7]}), nm);
    nm = MFMA8(ld4(kf + 2048), __builtin_bit_cast(s16x4, u32x2{phq[8], phq[9]}), nm);
    const float n8 = nm[8], pn8 = swap32(n8, hh);
    const float sa = gg[0] * __builtin_amdgcn_rcpf((hh ? n8 : pn8) + 1e-6f), sb = gg[1] * __builtin_amdgcn_rcpf(lsum);
    const int t = q0 + r;
    char* ob = ws + OFF_O + (size_t)(t >> 4) * 40960 + (t & 15) * 16;
    const int c0 = 5 * h + hh, c1 = c0 + 2, c2 = 5 * h + 4;
    *(u32x4*)(ob + c0 * 256) = u32x4{cvtpk(nm[0] * sa, nm[1] * sa), cvtpk(nm[2] * sa, nm[3] * sa), cvtpk(nm[4] * sa, nm[5] * sa), cvtpk(nm[6] * sa, nm[7] * sa)};
    *(u32x4*)(ob + c1 * 256) = u32x4{cvtpk(O[0] * sb, O[1] * sb), cvtpk(O[2] * sb, O[3] * sb), cvtpk(O[4] * sb, O[5] * sb), cvtpk(O[6] * sb, O[7] * sb)};
    if (!hh) *(u32x4*)(ob + c2 * 256) = u32x4{cvtpk(nm[8] * sa, nm[9] * sa), cvtpk(nm[10] * sa, nm[11] * sa), cvtpk(O[8] * sb, O[9] * sb), cvtpk(O[10] * sb, O[11] * sb)};
}

__global__ __launch_bounds__(256, 1) void p3_kernel(const char* ws, char* out) {
    __shared__ __attribute__((aligned(16))) char smem[4 * 20480];
    const int tid = threadIdx.x, lane = tid & 63;
    const int wid = __builtin_amdgcn_readfirstlane(tid >> 6);
    const int b = blockIdx.x, xcd = b & 7, slot = b >> 3;
    const int tt = (xcd >> 1) * 8 + (slot & 7), nq = (xcd & 1) * 4 + (slot >> 3);
    const char* ob = ws + OFF_O + (size_t)tt * 4 * 40960 + wid * 10240 + lane * 16;
    const char* wb = ws + OFF_WOF + (size_t)nq * 5 * 40960 + wid * 10240 + lane * 16;
    f32x4 acc[5][4];
#pragma unroll
    for (int i = 0; i < 5; ++i)
#pragma unroll
        for (int j = 0; j < 4; ++j) acc[i][j] = f32x4{};
    s16x8 af[10][5], bf[10][4];
#pragma unroll
    for (int ks = 0; ks < 10; ++ks) {
#pragma unroll
        for (int j = 0; j < 4; ++j) bf[ks][j] = *(const s16x8*)(ob + j * 40960 + ks * 1024);
#pragma unroll
        for (int i = 0; i < 5; ++i) af[ks][i] = *(const s16x8*)(wb + i * 40960 + ks * 1024);
    }
#pragma unroll
    for (int ks = 0; ks < 10; ++ks)
#pragma unroll
        for (int i = 0; i < 5; ++i)
#pragma unroll
            for (int j = 0; j < 4; ++j) acc[i][j] = MFMAS(af[ks][i], bf[ks][j], acc[i][j]);
    LDSP char* lds = (LDSP char*)smem;
#pragma unroll
    for (int i = 0; i < 5; ++i)
#pragma unroll
        for (int j = 0; j < 4; ++j) *(LDSP f32x4*)(lds + wid * 20480 + (i * 4 + j) * 1024 + lane * 16) = acc[i][j];
    __syncthreads();
#pragma unroll
    for (int i = 0; i < 5; ++i) {
        const LDSP char* p = lds + (i * 4 + wid) * 1024 + lane * 16;
        const f32x4 s = (*(const LDSP f32x4*)p + *(const LDSP f32x4*)(p + 20480)) + (*(const LDSP f32x4*)(p + 40960) + *(const LDSP f32x4*)(p + 61440));
        const int t = tt * 64 + wid * 16 + (lane & 15), n = nq * 80 + i * 16 + 4 * (lane >> 4);
        *(u32x2*)(out + (size_t)t * 1280 + n * 2) = u32x2{cvtpk(s[0], s[1]), cvtpk(s[2], s[3])};
    }
}

extern "C" void kernel_launch(void* const* d_in, const int* in_sizes, int n_in, void* d_out, int out_size, void* d_ws,
                              size_t ws_size, hipStream_t stream) {
    const char* x = (const char*)d_in[0];
    char* ws = (char*)d_ws;
    p1_kernel<<<256, 512, 0, stream>>>(x, (const char*)d_in[1], (const char*)d_in[5], (const char*)d_in[7],
                                       (const unsigned short*)d_in[3], (const unsigned short*)d_in[6], ws);
    p2_kernel<<<256, 512, 0, stream>>>((const char*)d_in[2], ws);
    p3_kernel<<<256, 256, 0, stream>>>(ws, (char*)d_out);
}
```

```cpp
#include <hip/hip_runtime.h>
#include <hip/hip_bf16.h>
#include <stdint.h>

typedef short s16x8 __attribute__((ext_vector_type(8)));
typedef short s16x4 __attribute__((ext_vector_type(4)));
typedef float f32x2 __attribute__((ext_vector_type(2)));
typedef float f32x4 __attribute__((ext_vector_type(4)));
typedef float f32x8 __attribute__((ext_vector_type(8)));
typedef float f32x16 __attribute__((ext_vector_type(16)));
typedef float f32x32 __attribute__((ext_vector_type(32)));
typedef unsigned u32x2 __attribute__((ext_vector_type(2)));
typedef unsigned u32x4 __attribute__((ext_vector_type(4)));
typedef unsigned long long u64;
typedef __bf16 bf16x2 __attribute__((ext_vector_type(2)));
typedef __bf16 bf16x8 __attribute__((ext_vector_type(8)));
typedef __bf16 bf16x16 __attribute__((ext_vector_type(16)));
typedef __amdgpu_buffer_rsrc_t rsrc_t;
#define LDSP __attribute__((address_space(3)))
#define GLBP __attribute__((address_space(1)))
#define DI __device__ __forceinline__
#define COMMA ,
#define TOUCHN 2
#define SBAR() __builtin_amdgcn_sched_barrier(0)
#define PIN(x) asm volatile("" : "+v"(x))
#define MFMA16(a, b, c) __builtin_amdgcn_mfma_f32_32x32x16_bf16((a), (b), (c), 0, 0, 0)
#define MFMA8(a, b, c) __builtin_amdgcn_mfma_f32_32x32x8bf16_1k((a), (b), (c), 0, 0, 0)
#define MFMA4B(a, b, c) __builtin_amdgcn_mfma_f32_32x32x4bf16_1k((a), (b), (c), 0, 0, 0)
#define SMFMA(a, b, c) __builtin_amdgcn_smfmac_f32_32x32x32_bf16(__builtin_bit_cast(bf16x8, a), __builtin_bit_cast(bf16x16, b), (c), 0xdddd, 0, 0)
#define MFMAS(a, b, c) __builtin_amdgcn_mfma_f32_16x16x32_bf16((a), (b), (c), 0, 0, 0)

constexpr unsigned QKB = 2560;
constexpr unsigned VBK = 2688;
constexpr unsigned STG = 8 * QKB + 8 * VBK;
constexpr unsigned VGS = 640 * 16 + 128;
constexpr unsigned VST = 64 * VGS, SEC_Q = 32 * 32 * QKB;
constexpr unsigned OFF_QA = 0, OFF_KA = SEC_Q, OFF_VA = 2 * SEC_Q, OFF_QB = OFF_VA + 4 * VST, OFF_KB = OFF_QB + SEC_Q, OFF_VB = OFF_KB + SEC_Q;
constexpr unsigned OFF_O = OFF_VB + 4 * VST;
constexpr unsigned OFF_WOF = OFF_O + 2048 * 1280 * 2;
constexpr unsigned OFF_G = OFF_WOF + 1280 * 640 * 2;
constexpr unsigned KVP = 4096;
constexpr unsigned OFF_KVP = OFF_G + 2048 * 8;
constexpr unsigned OFF_WPF = OFF_KVP + 32 * 8 * KVP;
#define FSTR 128
#define SLP 4
constexpr unsigned OFF_FLG = OFF_WPF + 32 * 1536;
constexpr unsigned FL_B = OFF_FLG, FL_A = FL_B + 32768, FL_KV = FL_A + 32768, FL_END = FL_KV + 32768;
constexpr unsigned OFF_EPO = FL_END;
constexpr unsigned WS_END = OFF_EPO + 128;
constexpr unsigned L_RING = 51 * 1024;
constexpr unsigned P2_V = 20480;
constexpr unsigned P2_SCR = 3 * STG, P2_KVF = P2_SCR + 8 * 3360 + 128, L_WORD = P2_KVF + 2048;
constexpr unsigned L_XCH = P2_KVF - 8 * 3072;
constexpr unsigned LDS_BYTES = 163840, L_VSRC = LDS_BYTES - 5120;
constexpr float LOG2E = 1.4426950408889634f;
constexpr float QSCALE = 0.22360679774997896f * 1.4426950408889634f;

template <int N> struct IC { static constexpr int v = N; };
template <int A, int B, class F> DI void sfor(F&& f) { if constexpr (A < B) { f(IC<A>{}); sfor<A + 1, B>(f); } }
DI unsigned cvtpk(float a, float b) { f32x2 v = {a, b}; return __builtin_bit_cast(unsigned, __builtin_convertvector(v, bf16x2)); }
DI float ex2(float x) { return __builtin_amdgcn_exp2f(x); }
template <bool DEV>
DI void glds16(unsigned voff, const void* sbase, unsigned ldsaddr) {
    if (DEV) asm volatile("s_nop 0\n\tglobal_load_lds_dwordx4 %0, %1 sc1" :: "v"(voff), "s"(sbase), "{m0}"(ldsaddr) : "memory");
    else asm volatile("s_nop 0\n\tglobal_load_lds_dwordx4 %0, %1" :: "v"(voff), "s"(sbase), "{m0}"(ldsaddr) : "memory");
}
#define WAITV(n) asm volatile("s_waitcnt vmcnt(%0)" :: "n"(n) : "memory")
#define WAITV_BAR(n) asm volatile("s_waitcnt vmcnt(%0) lgkmcnt(0)\n\ts_barrier" :: "n"(n) : "memory")
DI void st16(char* sbase, unsigned voff, u32x4 v) { asm volatile("global_store_dwordx4 %0, %1, %2 sc1\n\ts_nop 1" :: "v"(voff), "v"(v), "s"(sbase) : "memory"); }
DI void st8(char* sbase, unsigned voff, u32x2 v) { asm volatile("global_store_dwordx2 %0, %1, %2 sc1\n\ts_nop 1" :: "v"(voff), "v"(v), "s"(sbase) : "memory"); }
DI s16x8 dl16(rsrc_t rs, unsigned voff, unsigned soff) { return __builtin_bit_cast(s16x8, __builtin_amdgcn_raw_buffer_load_b128(rs, voff, soff, 16)); }
#define X_AUX3 16
DI s16x8 dl16p(rsrc_t rs, unsigned voff, unsigned soff) { return __builtin_bit_cast(s16x8, __builtin_amdgcn_raw_buffer_load_b128(rs, voff, soff, X_AUX3)); }
DI u32x2 dl8(rsrc_t rs, unsigned voff, unsigned soff) { return __builtin_bit_cast(u32x2, __builtin_amdgcn_raw_buffer_load_b64(rs, voff, soff, 16)); }
typedef short v4i16_t __attribute__((ext_vector_type(4)));
DI s16x4 trread(const LDSP char* p) { return __builtin_bit_cast(s16x4, __builtin_amdgcn_ds_read_tr16_b64_v4i16((LDSP v4i16_t*)p)); }
DI s16x8 cat8(s16x4 a, s16x4 b) { return __builtin_shufflevector(a, b, 0, 1, 2, 3, 4, 5, 6, 7); }
DI s16x8 ld8(const LDSP char* p) { return *(const LDSP s16x8*)p; }
DI s16x4 ld4(const LDSP char* p) { return *(const LDSP s16x4*)p; }

constexpr unsigned EMPTY = 0xffffffffu;
DI unsigned cvto(float a, float b) { return cvtpk(a, b); }
DI u64 gload(const char* ws, unsigned off) { return __hip_atomic_load((const GLBP u64*)(ws + off), __ATOMIC_RELAXED, __HIP_MEMORY_SCOPE_AGENT); }
DI u64 uni64(u64 v) { return (u64)__builtin_amdgcn_readfirstlane((unsigned)v) | ((u64)__builtin_amdgcn_readfirstlane((unsigned)(v >> 32)) << 32); }
DI void setflag(char* ws, unsigned off, u64 tag) { if ((threadIdx.x & 63) == 0) __hip_atomic_store((GLBP u64*)(ws + off), tag, __ATOMIC_RELAXED, __HIP_MEMORY_SCOPE_AGENT); }
DI bool allset(u64 v, u64 tag) { return __builtin_amdgcn_ballot_w64(v != tag) == 0; }
DI void waitflags(const char* ws, unsigned off, u64 tag) {
#pragma nounroll
    for (int i = 0; i < 4096; ++i) {
        if (allset(gload(ws, off), tag)) break;
        __builtin_amdgcn_s_sleep(SLP);
    }
    __builtin_amdgcn_fence(__ATOMIC_ACQUIRE, "wavefront");
}
DI void arrive(LDSP char* lds, int word, unsigned n, char* ws, unsigned off, u64 tag) {
    unsigned old, t = 1;
    asm volatile("s_mov_b64 exec, 1\n\tds_add_rtn_u32 %1, %2, %1\n\ts_waitcnt lgkmcnt(0)\n\tv_readfirstlane_b32 %0, %1\n\ts_mov_b64 exec, -1"
                 : "=s"(old), "+v"(t) : "v"((unsigned)(uintptr_t)lds + L_WORD + 4 * word) : "memory");
    if (old == n - 1) setflag(ws, off, tag);
}
#define ACQUIRE() do {} while (0)

#define STAMP_ARG
#define STAMP_PASS
#define STAMP_DECL do {} while (0)
#define STAMP(i) do {} while (0)
#define STAMP_OUT() do {} while (0)
#define XT 0
#define STAMP3(i) do { if (XT == 4) STAMP(i); } while (0)
#define STAMPT(n, i) do { if (XT == (n)) STAMP(i); } while (0)
#define GT 2
#define CSP 2
#define GTP 4
#define TAJ 9
#define VJ 58
#define PLJ 13
#define WJ 13
#define SPLIT 0
#define TR 1
#define DJ 66
#define ONE 1
#define AJ 52
#define ISP 0
#define HACK 0
#define EJ0 40
#define EJS 1
#define EJO 2
#define PJ1 65
#define CJ1 70
#define CJ2 77
static_assert(GTP != 0 && CJ1 + 7 <= 80 && (!ONE || PJ1 < 70) && (WJ - 38) / 8 < (VJ - 38) / 8 && VJ < CJ1 && EJ0 >= 40 && PJ1 >= 58 && CJ1 >= 70, "the counted waits assume this order");
#define LATE 1
#define PD 2
#define PUB 12
#define FRONT 1
#define OLDC 0
#define FC0 52
#define QS 1
#define LC1 108
#define LC2 124
#define AJO AJ
#define NEAR 1
#define LC0 55
#define LCS 2
constexpr int topj(int s) { return 38 + 8 * (s - 10); }
constexpr int cpos(int q) {
    return FRONT == 0 ? 2 * topj(10 + q / 4) + 1 + 2 * (q & 3) : q < 4 ? 76 + QS * q + (QS - 1) : FRONT == 1 ? (q < 8 ? (FC0 ? FC0 + 2 * (q - 4) : 76 + q) : 2 * LC0 + 1 + LCS * (q - 8))
         : (q < 8 ? LC1 : LC2) + LCS * (q & 3);
}
constexpr int ncp(int q, int y) { return OLDC && q >= 4 ? (y ? 0 : 2) : 1; }
constexpr int stj(int g, int y) { return EJ0 + g * EJS + (y ? EJO : 0); }
constexpr int arj(int y) { return y ? AJ : AJO; }
constexpr int between(int a, int b, int y) {
    int n = (159 > a && 159 < b) + (!ONE && 4 * PJ1 + 3 > a && 4 * PJ1 + 3 < b);
    for (int g = 0; g < 4; ++g) n += 4 * stj(g, y) + 3 > a && 4 * stj(g, y) + 3 < b;
    for (int q = 0; q < 12; ++q) n += 2 * cpos(q) > a && 2 * cpos(q) < b ? ncp(q, y) : 0;
    return n;
}
constexpr int fewest(int a, int b) { return between(a, b, 0) < between(a, b, 1) ? between(a, b, 0) : between(a, b, 1); }
constexpr int flight(int s) {
    return s <= 10 ? 4 + fewest(2 * (8 * s - 13), 4 * (4 * s - 2)) : fewest(s == 11 || (s == 13 && FC0) ? 150 : 2 * cpos(4 * (s - 12) + 3), 4 * topj(s));
}
constexpr int behind(int y) { return between(4 * stj(3, y) + 3, 4 * arj(y) + 3, y) + (!ONE && PJ1 == stj(3, y)); }
constexpr bool fits(int y) { return stj(3, y) < arj(y) && arj(y) + 1 < 70 && (arj(y) + 1 - 38) % 8 && arj(y) + 1 != WJ && arj(y) + 1 != VJ; }
static_assert(fits(0) && fits(1) && AJO <= AJ && EJO >= 0 && CSP == 2 && (FRONT == 1 || cpos(4) >= 2 * topj(11)) && cpos(7) < 2 * topj(13) && cpos(8) >= 2 * topj(12) && cpos(11) < 2 * topj(14) &&
              cpos(7) <= cpos(8) && FC0 % 2 == 0 && (FC0 == 0 || (FC0 >= 16 && FC0 + 6 < 76)) && (QS == 1 || FC0), "every store precedes the flag, every copy follows the barrier that frees its slot");
#define SPF 0
DI unsigned bflag(int h, int lane) { return FL_B + (((lane & 16 ? 10 : 5) + ((20 * h + (lane < 32 ? 0 : 19)) >> 7)) * 16 + (lane & 15)) * FSTR; }
DI bool bset(u32x4 v, u64 tag, int qb, int lane) {
    const unsigned lo = (unsigned)tag, hi = (unsigned)(tag >> 32);
    return __builtin_amdgcn_ballot_w64(((!NEAR || !(lane & 8)) && (v[0] != lo || v[1] != hi)) || (((lane & 30) == 2 * qb) && (v[2] != lo || v[3] != hi))) == 0;
}
DI unsigned vsrc(int h, int w, int k, int lane) {
    const unsigned p = (w * 5 + k) * 64 + lane, G = __umul24(p, 3121) >> 16, c = 8192 - k * 1024;
    unsigned a = __umul24(G, VGS - 336) + (p * 16 + 320 * h + c), b = ((20 * h) >> 7) * VGS + 10240 + c;
    asm("" : "+v"(a), "+s"(b));
    return p - __umul24(G, 21) < 20 ? a : b;
}
struct Vsrc { unsigned o[5]; };
DI const char* stage_src(const char* ws, int h, int st, int wid) {
    return wid < 4 ? ws + OFF_KB + h * (32 * QKB) + st * (8 * QKB) + wid * 5120 : ws + OFF_VB + st * VST - 8192;
}
DI void stage_all(const char* ws, int h, int st, int wid, int lane, const Vsrc& V, unsigned lds0) {
    const char* src = stage_src(ws, h, st, wid);
#pragma unroll
    for (int k = 0; k < 5; ++k) glds16<true>(V.o[k], src + k * 1024, lds0 + wid * 5120 + k * 1024);
}
DI Vsrc stage_lanes(int h, int wid, int lane) {
    Vsrc V;
#pragma unroll
    for (int k = 0; k < 5; ++k) V.o[k] = wid < 4 ? lane * 16 : vsrc(h, wid - 4, k, lane);
    return V;
}
DI void piece0(int k, const char* ws, int h, int qb, int wid, int lane, const Vsrc& V, unsigned lds0) {
    const char* gq = ws + OFF_QB + (h * 32 + qb * 4) * QKB + wid * 1024;
    if (k < 5) glds16<true>(V.o[k], stage_src(ws, h, 0, wid) + k * 1024, lds0 + wid * 5120 + k * 1024);
    else if (k == 5) glds16<true>(lane * 16, gq, lds0 + STG + wid * 1024);
    else if (wid == 0) glds16<true>(vsrc(h, 4, 0, lane), ws + OFF_VB - 8192, lds0 + 40960);
    else if (wid < 3) glds16<true>(lane * 16, gq + 7168, lds0 + STG + wid * 1024 + 7168);
}
DI void issue0(const char* ws, int h, int qb, int wid, int lane, const Vsrc& V, unsigned lds0) {
    ACQUIRE();
    stage_all(ws, h, 0, wid, lane, V, lds0);
    const char* gq = ws + OFF_QB + (h * 32 + qb * 4) * QKB + wid * 1024;
    glds16<true>(lane * 16, gq, lds0 + STG + wid * 1024);
    if (wid == 0) glds16<true>(vsrc(h, 4, 0, lane), ws + OFF_VB - 8192, lds0 + 40960);
    else if (wid < 3) glds16<true>(lane * 16, gq + 7168, lds0 + STG + wid * 1024 + 7168);
}

struct Dest { int lo[4], dh[4]; u64 m[4]; };
DI int runoff(int R) { const int u = (R * 205) >> 10, v = R - 5 * u; return (2 * u + ((v + 5) >> 3)) * (32 * QKB) + (((0x12 >> v) & 1) << 9); }
DI void st16m(char* sbase, unsigned voff, u32x4 v, u64 mask) {
    asm volatile("s_mov_b64 exec, %3\n\tglobal_store_dwordx4 %0, %1, %2 sc1\n\ts_mov_b64 exec, -1\n\ts_nop 0" :: "v"(voff), "v"(v), "s"(sbase), "s"(mask) : "memory");
}
template <bool VT>
DI u32x4 p1_cvt(const f32x16& a, int p) {
    if (VT) return u32x4{cvtpk(a[p], a[p + 1]), cvtpk(a[p + 2], a[p + 3]), cvtpk(a[p + 4], a[p + 5]), cvtpk(a[p + 6], a[p + 7])};
    return u32x4{cvtpk(a[p], a[p + 1]), cvtpk(a[p + 2], a[p + 3]), cvtpk(a[p + 4], a[p + 5]), cvtpk(a[p + 6], a[p + 7])};
}
template <bool VT>
DI u32x4 p1_swap(u32x4 c, u64 m) {
    if (VT) return c;
    auto x = __builtin_amdgcn_permlane32_swap(c[0], c[2], false, false);
    auto y = __builtin_amdgcn_permlane32_swap(c[1], c[3], false, false);
    unsigned x0 = x[0], x1 = x[1], y0 = y[0], y1 = y[1];
    asm volatile("s_nop 0\n\ts_mov_b64 exec, %4\n\tv_permlane16_swap_b32 %0, %1\n\tv_permlane16_swap_b32 %2, %3\n\ts_mov_b64 exec, -1"
                 : "+v"(x0), "+v"(x1), "+v"(y0), "+v"(y1) : "s"(m));
    return u32x4{x0, y0, x1, y1};
}
template <bool VT>
DI void p1_pack(const f32x16 (&acc)[2], const Dest& D, u32x4 (&P)[4]) {
#pragma unroll
    for (int g = 0; g < 4; ++g) P[g] = p1_swap<VT>(p1_cvt<VT>(acc[g >> 1], 8 * (g & 1)), D.m[g]);
}
DI void bst16(rsrc_t rs, unsigned voff, unsigned soff, u32x4 v) {
    asm volatile("buffer_store_dwordx4 %0, %1, %2, %3 offen sc1\n\ts_nop 1" :: "v"(v), "v"(voff), "s"(rs), "s"(soff) : "memory");
}
template <bool VT>
DI void p1_dest(int g, unsigned dst, const Dest& D, unsigned b16, unsigned b8, int hh, unsigned& vo, unsigned& so) {
    if (VT) { vo = b16; so = dst + (g & 1) * (2 * VGS) + (g >> 1) * 512; }
    else {
        vo = b16 + ((unsigned)(D.m[g] >> 32) & 512) + __mul24(hh, D.dh[g]);
        asm("v_cndmask_b32 %0, %0, %1, %2" : "+v"(vo) : "v"(b8), "s"(D.m[g]));
        so = dst + D.lo[g];
    }
}
template <bool VT>
DI void p1_emit(int g, u32x4 P, rsrc_t rs, unsigned dst, const Dest& D, unsigned b16, unsigned b8, int hh) {
    unsigned vo, so;
    p1_dest<VT>(g, dst, D, b16, b8, hh, vo, so);
    bst16(rs, vo, so, P);
}
struct Late { u32x4 P[4]; unsigned vo[4]; bool four; };
DI void p2_enter(const char* ws, rsrc_t rs, int h, int qb, int wid, int lane, const Vsrc& V, unsigned lds0, bool early, u64 tag STAMP_ARG) {
    __builtin_amdgcn_s_setprio(3);
    if (__builtin_expect(!early, 0)) {
#pragma nounroll
        for (int i = 0; i < 4096; ++i) {
            if (bset(__builtin_bit_cast(u32x4, dl16(rs, bflag(h, lane), 0)), tag, qb, lane)) break;
            __builtin_amdgcn_s_sleep(SLP);
            asm volatile("" ::: "memory");
        }
        issue0(ws, h, qb, wid, lane, V, lds0);
    }
    STAMP(7);
}

DI rsrc_t wsrc(char* ws) {
    asm volatile("" : "+s"(ws));
    return __builtin_amdgcn_make_buffer_rsrc(ws, 0, WS_END, 0x00020000);
}
constexpr int ahead(int J) { return J + (J < 42 ? 2 : 2 + (J - 40) / 2 < PD ? 2 + (J - 40) / 2 : PD); }
struct Head { unsigned gx[2], gw; const char* xb; };
DI Head p1_head(const char*& x, const char*& wB, const char*& wA, int mt, int ct, int c0, LDSP char* lds) {
    const int lane = threadIdx.x & 63, wid = __builtin_amdgcn_readfirstlane(threadIdx.x >> 6);
    const unsigned ring = (unsigned)(uintptr_t)lds + L_RING + wid * 2048;
    unsigned gx[2];
#pragma unroll
    for (int i = 0; i < 2; ++i) gx[i] = (16 * wid + 8 * i + (lane >> 3)) * 1280 + (((lane & 7) ^ (4 * i + (lane >> 4))) << 4);
    unsigned gw = (8 * wid + ((lane >> 2) & 3)) * 3840 + (lane >> 4) * 64 + (lane & 3) * 16;
    asm volatile("" : "+v"(gx[0]), "+v"(gx[1]), "+v"(gw));
    asm volatile("" : "+s"(x), "+s"(wB));
    wB += ct * 256; wA += ct * 256;
    const char* xb = x + (size_t)mt * 128 * 1280;
    __builtin_amdgcn_s_setprio(3);
#pragma unroll
    for (int i = 0; i < 2; ++i) glds16<false>(gx[i], xb + c0 * 128, ring + i * 1024);
#pragma unroll
    for (int i = 0; i < 2; ++i) glds16<false>(gw, wB + (c0 * (64 * 3840) + i * (4 * 3840)), ring + 16384 + i * 1024);
    __builtin_amdgcn_s_setprio(0);
    return Head{{gx[0], gx[1]}, gw, xb};
}
template <bool VT>
DI void p1_tile(const Head& H, const char* x, const char* wB, const char* wA, char* ws, rsrc_t& rs, int mt, int ct, int c0, int h2, int qb2, LDSP char* lds, u64& tag, Vsrc& V, Late& L STAMP_ARG) {
    const int tid = threadIdx.x, lane = tid & 63, r = lane & 31, hh = lane >> 5;
    const int wid = __builtin_amdgcn_readfirstlane(tid >> 6), wt = wid >> 1, wc = wid & 1;
    const unsigned lds0 = (unsigned)(uintptr_t)lds, ring = lds0 + L_RING + wid * 2048;
    const unsigned (&gx)[2] = H.gx;
    const unsigned gw = H.gw;
    const char* xb = H.xb;
    auto half = [&](int n, int i, int w = 0) {
        const unsigned dst = ring + (FRONT == 1 && n / 2 == 13 ? (n - 26) * 16384 - L_RING : (n % 6) * 16384) + i * 1024 + w * 2048;
        const int c = n < 20 ? n >> 1 : n - 20, k = c + c0 - (c + c0 >= 10 ? 10 : 0);
        if (n < 20 && !(n & 1)) glds16<false>(gx[i], xb + k * 128, dst);
        else glds16<false>(gw, (n < 20 ? wB : wA) + (k * (64 * 3840) + i * (4 * 3840) + w * (8 * 3840)), dst);
    };
    auto item = [&](int n) { half(n, 0); half(n, 1); };
    auto fetch = [&](const char* p) { asm volatile("s_nop 0\n\tglobal_load_lds_dword %0, off" :: "v"(p), "{m0}"(lds0 + L_RING + 6 * 16384 + (wid & 3) * 256) : "memory"); };
    auto touch = [&]() {
        if (GT == 1 && wid < 4) {
            const int id = ((ct * 16 + mt) * 4 + wid) * 64 + lane;
            fetch(id < 20480 ? x + id * 128 : id < 39680 ? wB - ct * 256 + (id - 20480) * 128 : wA - ct * 256 + (id < 58880 ? id - 39680 : 0) * 128);
        }
        if (GT == 2 && wid < 2) {
            const int id = ((ct * 16 + mt) * 2 + wid) * 64 + lane, row = (id * 18725) >> 17;
            fetch(id < 14336 ? x + (id + 3 * row + 3) * 128 : wB - ct * 256 + (id < 27776 ? id - 14336 + 192 * 30 : 0) * 128);
        }
    };
    auto touchA = [&]() {
        if (GT == 2 && (wid & ~1) == 2) { const int id = ((ct * 16 + mt) * 2 + (wid & 1)) * 40 + (lane < 40 ? lane : 0); fetch(wA - ct * 256 + id * 128); }
    };
    if (GTP == 0) touch();
    rs = wsrc(ws);
    if (tid < 8) *(LDSP unsigned*)(lds + L_WORD + 4 * tid) = 0u;
    const LDSP char* xr[4];
#pragma unroll
    for (int ks = 0; ks < 4; ++ks) xr[ks] = lds + L_RING + (32 * wt + r) * 128 + ((((unsigned)(hh ^ ((r >> 1) & 7))) << 4) ^ (ks << 5));
    const LDSP char* wr0 = lds + L_RING + hh * 2048 + wc * 512 + ((lane & 15) >> 2) * 64 + ((lane >> 4) & 1) * 32 + (lane & 3) * 8;
    const LDSP char* wr1 = wr0 + 3 * 16384;
    const LDSP char* wr2 = wr0;
    PIN(wr0); PIN(wr1); PIN(xr[0]); PIN(xr[1]); PIN(xr[2]); PIN(xr[3]);
    s16x8 xf[40], wf[PD + 1][2];
    f32x16 acc[2];
    auto rdw = [&](int J, int c) {
        const int s = (J < 40 ? 2 * (J >> 2) + 1 : 20 + ((J - 40) >> 2)) % 6;
        const LDSP char* p = (FRONT == 1 && J / 8 == 8 ? wr2 + ((J >> 2) & 1) * 16384 : (s < 3 ? wr0 : wr1) + (s % 3) * 16384) + (J & 3) * 4096 + c * 256;
        wf[J % (PD + 1)][c] = cat8(trread(p), trread(p + 1024));
    };
    auto mm = [&](int J, int c) {
        const f32x16 z = J % 40 == 0 ? f32x16{} : acc[c];
        acc[c] = VT ? MFMA16(xf[J % 40], wf[J % (PD + 1)][c], z) : MFMA16(wf[J % (PD + 1)][c], xf[J % 40], z);
        SBAR();
    };
    auto rdx = [&](int J) { xf[J] = ld8(xr[J & 3] + ((J >> 2) % 3) * 32768); };
    const int sec = ct / 5, colbase = (ct - 5 * sec) * 128 + wc * 64, tp = mt * 2 + (wt >> 1);
    Dest D;
    auto plan = [&]() {
        int cb = colbase >> 3;
        asm volatile("" : "+s"(cb));
#pragma unroll
        for (int g = 0; g < 4; ++g) {
            const int R0 = cb + 2 * g, v0 = R0 - 5 * ((R0 * 205) >> 10), o0 = runoff(R0), o1 = runoff(R0 + 1);
            D.lo[g] = o0 - (v0 == 1 ? 512 : 0); D.dh[g] = o1 - o0;
            D.m[g] = v0 == 1 ? 0xffffffff00000000ull : v0 == 2 ? 0xffffffffull : 0ull;
            asm volatile("" : "+s"(D.lo[g]), "+s"(D.dh[g]), "+s"(D.m[g]));
        }
    };
    if (!VT && PLJ < 0) plan();
    const unsigned b16 = VT ? hh * VGS + r * 16 : r * 16, b8 = 2048 - (wt & 1) * 768 + (r & 15) * 16 + (r >> 4) * (32 * QKB);
    unsigned dstA = VT ? OFF_VA + (tp >> 3) * VST + ((tp & 7) * 2 + (wt & 1)) * (4 * VGS) + colbase * 16 : sec * SEC_Q + tp * QKB + (wt & 1) * 1024;
    dstA = __builtin_amdgcn_readfirstlane(dstA);
    asm volatile("" : "+s"(dstA));
    STAMPT(8, 26);
    if (GTP == 1) touch();
    item(2); item(3);
    if (GTP == 2) touch();
    if (WJ < 0 && wid < 4)
#pragma unroll
        for (int k = 0; k < 5; ++k) *(LDSP unsigned*)(lds + L_VSRC + k * 1024 + tid * 4) = vsrc(h2, wid, k, lane);
    STAMPT(8, 27);
    if (GT && (GTP == 1 || GTP == 2) && wid < (GT == 1 ? 4 : 2)) WAITV_BAR(5); else WAITV_BAR(4);
    STAMP(1);
    rdx(0); rdw(0, 0); rdw(0, 1);
    rdx(1); rdw(1, 0); rdw(1, 1);
    u64 epo;
    u32x4 P[4];
    u32x4 fl, fl2;
    const unsigned fo = bflag(h2, lane);
    bool early = false, first = false;
    unsigned cnt, seen;
    auto copy = [&](auto mc) {
        constexpr int m = decltype(mc)::v;
        if constexpr (m < 4) { SBAR(); if (m == 0 && GTP == 3) touch(); half(4 + m / 2, m & 1); if (m == 3 && GTP == 4) touch(); }
        sfor<0, 12>([&](auto qc) __attribute__((always_inline)) {
            constexpr int q = decltype(qc)::v;
            if constexpr (m == cpos(q)) {
                SBAR();
                if (ncp(q, 1)) half(24 + q / 2, q & 1);
                else if (wid < 4) { half(24 + q / 2, q & 1); half(24 + q / 2, q & 1, 4); }
            }
        });
        sfor<1, 10>([&](auto sc) __attribute__((always_inline)) {
            constexpr int s = decltype(sc)::v, m0 = 2 * (s < 10 ? 4 * s - 2 : 8 * s - 42) + 1;
            if constexpr (CSP == 0 && m == m0) { SBAR(); item(2 * s + 4); item(2 * s + 5); }
            if constexpr (CSP != 0 && m >= m0 && (m - m0) % CSP == 0 && (m - m0) / CSP < 4) { SBAR(); half(2 * s + 4 + (m - m0) / CSP / 2, (m - m0) / CSP & 1); }
        });
    };
    auto conv = [&](int c) {
        if (!VT && sec == 0) acc[c] *= QSCALE;
        P[2 * c] = p1_cvt<VT>(acc[c], 0); P[2 * c + 1] = p1_cvt<VT>(acc[c], 8);
        PIN(P[2 * c]); PIN(P[2 * c + 1]);
        SBAR();
    };
    auto dest = [&]() {
#pragma unroll
        for (int g = 0; g < 4; ++g) {
            unsigned so;
            p1_dest<VT>(g, dstA, D, b16, b8, hh, L.vo[g], so);
            L.vo[g] += so;
        }
    };
    static_assert(PD == 2, "copy() knows where the steps begin");
    sfor<0, 80>([&](auto jc) __attribute__((always_inline)) {
        constexpr int J = decltype(jc)::v;
        constexpr int R0 = ahead(J), R1 = ahead(J + 1);
        constexpr int T = R0 < 40 ? (R0 + 3) / 4 * 4 : 40 + (R0 - 40 + 7) / 8 * 8, s = T < 40 ? T / 4 : 10 + (T - 40) / 8;
        constexpr bool top = T < R1 && T < 80;
        static_assert(!top || T == R0, "a step starts with its first read");
        mm(J, 0);
        if (top) {
            if (s >= 10 && XT == 5) STAMP((14 + s) % 29);
            if (s >= 5 && s < 10 && XT == 6) STAMP((19 + s) % 29);
            if (s >= 1 && s < 6 && XT == 7) STAMP((23 + s) % 29);
            if (ONE && s == 14 && wid == 0) {
                asm volatile("s_waitcnt vmcnt(0)" : "+{v[248:251]}"(fl) :: "memory");
                *(volatile LDSP unsigned*)(lds + L_WORD + 16) = bset(fl, tag, qb2, lane);
            }
            if (s == 1 && GT && GTP >= 2 && GTP <= 4) { if (wid < (GT == 1 ? 4 : 2)) WAITV_BAR(5); else WAITV_BAR(4); } else
            if (s == 2 && GT && GTP == 4) { if (wid < (GT == 1 ? 4 : 2)) WAITV_BAR(5); else WAITV_BAR(4); } else
            WAITV_BAR(flight(s));
            if (s >= 10 && XT == 5) STAMP((8 + s) % 29);
            if (ONE && s == 14) seen = *(volatile LDSP unsigned*)(lds + L_WORD + 16);
        }
        if (R0 < 40) rdx(R0);
        if (R0 < 80) rdw(R0, 0);
        if (R0 + 1 < R1 && R0 + 1 < 80) rdw(R0 + 1, 0);
        copy(IC<2 * J>{});
        SBAR();
        if (SPLIT && J == 40) conv(1);
        if (!TR || J < 77) mm(J, 1);
        if (R0 < 80) rdw(R0, 1);
        if (R0 + 1 < R1 && R0 + 1 < 80) rdw(R0 + 1, 1);
        copy(IC<2 * J + 1>{});
        if (J == TAJ) touchA();
        if (!VT && J == PLJ) plan();
        if (J == 39) {
            STAMP(2);
            if (SPLIT) conv(0);
            else {
                if (!VT && sec == 0) { acc[0] *= QSCALE; acc[1] *= QSCALE; }
                p1_pack<VT>(acc, D, P);
            }
            if (VT && wid == 0) st16m(ws + OFF_VB + (mt >> 2) * VST + (ct - 10) * VGS + 10240, 0, u32x4{0x3f803f80u, 0x3f803f80u, 0x3f803f80u, 0x3f803f80u}, 1ull);
            asm volatile("global_load_dwordx2 %0, %1, %2 sc1" : "=&{v[254:255]}"(epo) : "v"(0), "s"(ws + OFF_EPO) : "memory");
            STAMP(3);
        }
#pragma unroll
        for (int g = 0; g < 4; ++g) {
            const bool a = J == stj(g, 0), b = J == stj(g, 1);
            if (a || b) SBAR();
            if ((a && b) || (a && wid < 4) || (b && wid >= 4)) p1_emit<VT>(g, SPLIT ? p1_swap<VT>(P[g], D.m[g]) : P[g], rs, dstA + OFF_QB, D, b16, b8, hh);
        }
        sfor<0, 2>([&](auto yc) __attribute__((always_inline)) {
            constexpr int y = decltype(yc)::v;
            if (y && AJ == AJO) return;
            const bool mine = AJ == AJO || (wid >= 4) == y;
            if (J == arj(y) && mine) {
                WAITV(AJ != AJO || behind(0) < behind(1) ? behind(y) : behind(1));
                cnt = 1;
                asm volatile("s_mov_b64 exec, 1\n\tds_add_rtn_u32 %0, %1, %0\n\ts_mov_b64 exec, -1" : "+v"(cnt) : "v"(lds0 + L_WORD + 12) : "memory");
            }
            if (!y && J == AJO) {
                if (AJ != AJO) WAITV(fewest(159, 4 * AJO + 3));
                asm volatile("" : "+{v[254:255]}"(epo));
                tag = uni64(epo) + 1;
            }
            if (J == arj(y) + 1 && mine) {
                asm volatile("s_waitcnt lgkmcnt(4)" : "+v"(cnt) :: "memory");
                if (__builtin_amdgcn_readfirstlane(cnt) == 7) setflag(ws, FL_B + ((sec ? ct : 5 + ct) * 16 + mt) * FSTR + (sec ? 0 : 8), tag);
                STAMP(4);
            }
        });
        if (J == WJ && wid < 4) {
            int l2 = lane;
            asm volatile("" : "+v"(l2));
#pragma unroll
            for (int k = 0; k < 5; ++k) *(LDSP unsigned*)(lds + L_VSRC + k * 1024 + tid * 4) = vsrc(h2, wid, k, l2);
        }
        if (J == VJ) {
#pragma unroll
            for (int k = 0; k < 5; ++k) { V.o[k] = *(const LDSP unsigned*)(lds + L_VSRC + k * 1024 + (tid & 255) * 4); if (wid < 4) V.o[k] = lane * 16; }
        }
        if (J == DJ) { dest(); PIN(L.vo[0]); PIN(L.vo[1]); PIN(L.vo[2]); PIN(L.vo[3]); }
        if (FRONT == 1 && J == 60) { wr2 = wr0 - L_RING; PIN(wr2); }
        if (J == PJ1 && (!ONE || wid == 0)) asm volatile("global_load_dwordx4 %0, %1, %2 sc1" : "=&{v[248:251]}"(fl) : "v"(fo), "s"(ws) : "memory");
        if (J == CJ1) {
            if (ONE) early = __builtin_amdgcn_readfirstlane(seen) != 0;
            else {
                asm volatile("s_waitcnt vmcnt(0)" : "+{v[248:251]}"(fl) :: "memory");
                early = bset(fl, tag, qb2, lane);
            }
            first = early;
            if (early) { if (!ISP) issue0(ws, h2, qb2, wid, lane, V, lds0); }
            else if (CJ1 < CJ2) asm volatile("global_load_dwordx4 %0, %1, %2 sc1" : "=&{v[252:255]}"(fl2) : "v"(fo), "s"(ws) : "memory");
        }
        if (ISP && J >= CJ1 && J < CJ1 + 7 && first) piece0(J - CJ1, ws, h2, qb2, wid, lane, V, lds0);
        if (CJ1 < CJ2 && J == CJ2 && !early) {
            asm volatile("s_waitcnt vmcnt(0)" : "+{v[252:255]}"(fl2) :: "memory");
            early = bset(fl2, tag, qb2, lane);
            if (early) issue0(ws, h2, qb2, wid, lane, V, lds0);
        }
        SBAR();
    });
    if (!DJ) dest();
    auto done = [&](int g) { L.P[g] = p1_swap<VT>(p1_cvt<VT>(acc[g >> 1], 8 * (g & 1)), D.m[g]); };
    if (TR) {
        mm(77, 1); done(0); PIN(L.P[0]); SBAR();
        mm(78, 1); done(1); PIN(L.P[1]); SBAR();
        mm(79, 1);
        STAMP(5);
        if (wid < 4) { bst16(rs, L.vo[0], 0, L.P[0]); bst16(rs, L.vo[1], 0, L.P[1]); }
        done(2); done(3);
        if (wid < 4) { bst16(rs, L.vo[2], 0, L.P[2]); bst16(rs, L.vo[3], 0, L.P[3]); }
    } else {
        STAMP(5);
#pragma unroll
        for (int g = 0; g < 4; ++g) done(g);
        if (wid < 4)
#pragma unroll
            for (int g = 0; g < 4; ++g) bst16(rs, L.vo[g], 0, L.P[g]);
    }
    L.four = early;
    STAMP(6);
    p2_enter(ws, rs, h2, qb2, wid, lane, V, lds0, early, tag STAMP_PASS);
}

DI void p1_spare(const char* x, const char* wA, const char* gate_w, const char* wphi, const char* woA, const char* woB, char* ws, int sp, LDSP char* lds, u64& tag STAMP_ARG) {
    const int tid = threadIdx.x, lane = tid & 63, wid = tid >> 6;
    if (SPF) {
        const int q = sp >> 2, e = 96 + q * 6, wu = __builtin_amdgcn_readfirstlane(wid);
        const unsigned dst = (unsigned)(uintptr_t)lds + 76800 + wu * 256;
#pragma unroll 1
        for (int k = 0; k < 5; ++k) {
#pragma unroll
            for (int i = 0; i < 2; ++i) {
                const int n = ((sp & 1) * 8 + wu) * 80 + i * 64 + lane, row = k * 128 + n / 10, m = n % 10, t = m >> 1;
                const int ct = t < 3 ? 3 * q + t : (t == 3 ? e : e + 5) >> 3;
                const u64 mask = i ? 0xffffull : ~0ull;
                asm volatile("s_mov_b64 exec, %3\n\tglobal_load_lds_dword %0, %1\n\ts_mov_b64 exec, -1" :: "v"(row * 3840 + ct * 256 + (m & 1) * 128), "s"(wA), "{m0}"(dst), "s"(mask) : "memory");
            }
        }
    }
    const u64 epo = gload(ws, OFF_EPO);
    if (tid < 8) *(LDSP unsigned*)(lds + L_WORD + 4 * tid) = 0u;
    u32x4 wr[2][8];
#pragma unroll
    for (int it = 0; it < 2; ++it) {
        const int id = sp * 800 + (it * 512 + tid < 800 ? it * 512 + tid : 0), ch = id / 80, oc = id - ch * 80;
        const int g = ch / 20, q = ch - 20 * g, h = 4 * g + (q < 16 ? (q & 7) >> 1 : q - 16), i = q < 16 ? (q >> 3) * 2 + (q & 1) : 4;
        const char* p0 = (i == 2 || i == 3 ? woB : woA) + (h * 20 + (i == 4 ? 16 : 4 * (i & 1))) * 1280 + oc * 16;
        const char* p1 = i == 4 ? woB + (h * 20 + 16) * 1280 + oc * 16 : p0 + 8 * 1280;
#pragma unroll
        for (int j = 0; j < 4; ++j) { wr[it][j] = *(const u32x4*)(p0 + j * 1280); wr[it][4 + j] = *(const u32x4*)(p1 + j * 1280); }
    }
    u32x4 gw[5], xv[4][5];
#pragma unroll
    for (int i = 0; i < 5; ++i) gw[i] = *(const u32x4*)(gate_w + ((lane & 15) + 16 * i) * 16);
#pragma unroll
    for (int it = 0; it < 4; ++it)
#pragma unroll
        for (int i = 0; i < 5; ++i) xv[it][i] = *(const u32x4*)(x + (size_t)(sp * 128 + wid * 16 + it * 4 + (lane >> 4)) * 1280 + ((lane & 15) + 16 * i) * 16);
    if (wid < 2) {
        const int h = 2 * sp + wid, r = lane & 31, hh = lane >> 5, f = r < 18 ? r : (r == 20 || r == 21) ? r - 2 : 0;
        const unsigned short* wp = (const unsigned short*)wphi + h * 400 + f;
        unsigned v[12];
#pragma unroll
        for (int j = 0; j < 8; ++j) v[j] = wp[(8 * hh + 4 * (h & 1) + j) * 20];
#pragma unroll
        for (int j = 0; j < 4; ++j) v[8 + j] = wp[((h & 1 ? 0 : 16) + j) * 20];
        st16(ws, OFF_WPF + h * 1536 + lane * 16, u32x4{v[0] | (v[1] << 16), v[2] | (v[3] << 16), v[4] | (v[5] << 16), v[6] | (v[7] << 16)});
        st8(ws, OFF_WPF + h * 1536 + 1024 + lane * 8, u32x2{hh ? 0u : v[8] | (v[9] << 16), hh ? 0u : v[10] | (v[11] << 16)});
    }
#pragma unroll
    for (int it = 0; it < 2; ++it) {
        LDSP char* tr = lds + wid * 9216;
#pragma unroll
        for (int c = 0; c < 8; ++c) {
            const unsigned sel = (c & 1) ? 0x07060302u : 0x05040100u;
            *(LDSP u32x4*)(tr + lane * 144 + c * 16) = u32x4{__builtin_amdgcn_perm(wr[it][1][c >> 1], wr[it][0][c >> 1], sel), __builtin_amdgcn_perm(wr[it][3][c >> 1], wr[it][2][c >> 1], sel),
                                                             __builtin_amdgcn_perm(wr[it][5][c >> 1], wr[it][4][c >> 1], sel), __builtin_amdgcn_perm(wr[it][7][c >> 1], wr[it][6][c >> 1], sel)};
        }
#pragma unroll
        for (int j = 0; j < 8; ++j) {
            const int li = it * 512 + wid * 64 + j * 8 + (lane >> 3), id = sp * 800 + li, ch = id / 80, oc = id - ch * 80;
            const u32x4 v = *(const LDSP u32x4*)(tr + (j * 8 + (lane >> 3)) * 144 + (lane & 7) * 16);
            if (li < 800) st16(ws, OFF_WOF + ((((oc / 10) * 40 + (ch >> 2)) * 5 + (oc % 10 >> 1)) * 64 + (ch & 3) * 16 + (oc & 1) * 8 + (lane & 7)) * 16, v);
        }
    }
    float gl = 0.f;
#pragma unroll
    for (int it = 0; it < 4; ++it) {
        float acc = 0.f;
#pragma unroll
        for (int i = 0; i < 5; ++i)
#pragma unroll
            for (int j = 0; j < 4; ++j) { const unsigned a = xv[it][i][j], b = gw[i][j]; acc = __builtin_amdgcn_fdot2_f32_bf16(__builtin_bit_cast(bf16x2, a), __builtin_bit_cast(bf16x2, b), acc, false); }
#pragma unroll
        for (int m = 1; m < 16; m <<= 1) acc += __shfl_xor(acc, m, 64);
        if ((lane & 15) == it) gl = acc;
    }
    const float g = 1.f / (1.f + __expf(-gl));
    if ((lane & 15) < 4) st8(ws, OFF_G + (sp * 128 + wid * 16 + (lane & 15) * 4 + (lane >> 4)) * 8, u32x2{__float_as_uint(g), __float_as_uint(1.f - g)});
    STAMP(5);
    WAITV_BAR(0);
    tag = uni64(epo) + 1;
    if (tid < 64) setflag(ws, FL_A + (240 + sp) * FSTR, tag);
    STAMP(6);
}
#define RED2(a, b, OP) do { \
    auto r_ = __builtin_amdgcn_permlane32_swap(__float_as_uint(a), __float_as_uint(b), false, false); \
    const unsigned z_ = __float_as_uint(OP(__uint_as_float(r_[0]), __uint_as_float(r_[1]))); \
    auto s_ = __builtin_amdgcn_permlane32_swap(z_, z_, false, false); \
    a = __uint_as_float(s_[0]); b = __uint_as_float(s_[1]); } while (0)
#define FADD(x, y) ((x) + (y))
#define F0 6
#define F1 2
#define FMAX(x, y) __builtin_elementwise_maximum(x, y)
#define FMIN(x, y) __builtin_elementwise_minimum(x, y)
DI float one(float x) { asm("" : "+v"(x)); return x; }
DI void features(s16x8 f1, s16x4 f2, s16x8 w1, s16x4 w2, f32x2 (&e)[10], float& sp, float& sn) {
    f32x16 p = MFMA16(w1, f1, f32x16{});
    p = MFMA8(w2, f2, p);
#pragma unroll
    for (int i = 0; i < 10; i += 2) { const f32x2 t = f32x2{p[i], p[i + 1]} * LOG2E; p[i] = t[0]; p[i + 1] = t[1]; }
    float am = FMAX(FMAX(fabsf(p[0]), fabsf(p[1])), fabsf(p[2])), an = FMAX(FMAX(fabsf(p[3]), fabsf(p[4])), fabsf(p[5]));
    PIN(am); PIN(an);
    am = FMAX(FMAX(am, fabsf(p[6])), fabsf(p[7])); an = FMAX(FMAX(an, fabsf(p[8])), fabsf(p[9]));
    PIN(am); PIN(an);
    if (__builtin_expect(__builtin_amdgcn_ballot_w64(!(FMAX(am, an) <= 48.f)) == 0, 1)) {
#pragma unroll
        for (int i = 0; i < 10; ++i) { e[i >> 1][i & 1] = ex2(p[i]); e[5 + (i >> 1)][i & 1] = ex2(-p[i]); }
    } else {
        float mx = FMAX(p[0], p[1]), mn = FMIN(p[0], p[1]);
#pragma unroll
        for (int i = 2; i < 10; i += 2) { mx = FMAX(FMAX(mx, p[i]), p[i + 1]); mn = FMIN(FMIN(mn, p[i]), p[i + 1]); }
        mx = -mx;
        RED2(mx, mn, FMIN);
#pragma unroll
        for (int i = 0; i < 10; ++i) { e[i >> 1][i & 1] = ex2(p[i] + mx); e[5 + (i >> 1)][i & 1] = ex2(mn - p[i]); }
    }
    const f32x2 s2 = ((e[0] + e[1]) + (e[2] + e[3])) + e[4], t2 = ((e[5] + e[6]) + (e[7] + e[8])) + e[9];
    sp = one(s2[0] + s2[1]); sn = one(t2[0] + t2[1]);
    RED2(sp, sn, FADD);
}
DI f32x32 cat32(f32x16 a, f32x16 b) {
    return __builtin_shufflevector(a, b, 0, 1, 2, 3, 4, 5, 6, 7, 8, 9, 10, 11, 12, 13, 14, 15, 16, 17, 18, 19, 20, 21, 22, 23, 24, 25, 26, 27, 28, 29, 30, 31);
}
DI void rowmax2(const f32x32& s, float& ma, float& mb) {
#pragma unroll
    for (int i = 0; i < 16; i += 2) { ma = fmaxf(fmaxf(ma, s[i]), s[i + 1]); mb = fmaxf(fmaxf(mb, s[16 + i]), s[17 + i]); }
}

#define ASM_0A \
    "s_setprio 2\n\ts_waitcnt lgkmcnt(0)\n\tv_mfma_f32_32x32x16_bf16 v[96:111], v[204:207], v[192:195], v[160:175]\n\tv_exp_f32 v64, v64\n\t" \
    "v_exp_f32 v65, v65\n\tv_exp_f32 v66, v66\n\tds_read_b128 v[216:219], %[va] offset:1344\n\tds_read_b128 v[220:223], %[va] offset:2016\n\t" \
    "v_exp_f32 v67, v67\n\tv_exp_f32 v68, v68\n\tv_exp_f32 v69, v69\n\tv_exp_f32 v70, v70\n\t" \
    "v_mfma_f32_32x32x16_bf16 v[112:127], v[204:207], v[196:199], v[176:191]\n\tds_read_b128 v[204:207], %[ka] offset:2560\n\tv_exp_f32 v71, v71\n\t" \
    "v_exp_f32 v72, v72\n\tv_exp_f32 v73, v73\n\tv_exp_f32 v74, v74\n\tv_smfmac_f32_32x32x32_bf16 v[128:143], v[208:211], v[64:71], v224\n\t" \
    "global_load_lds_dwordx4 %[dv0], %[dg] offset:0 sc1\n\tv_exp_f32 v75, v75\n\tv_exp_f32 v76, v76\n\tv_exp_f32 v77, v77\n\tv_exp_f32 v78, v78\n\t" \
    "v_mfma_f32_32x32x4_2b_bf16 v[96:127], v[202:203], v[200:201], v[96:127]\n\tds_read_b64 v[202:203], %[k2a] offset:2560\n\tv_exp_f32 v79, v79\n\t" \
    "v_exp_f32 v80, v80\n\tv_exp_f32 v81, v81\n\tv_exp_f32 v82, v82\n\tv_exp_f32 v83, v83\n\tv_exp_f32 v84, v84\n\tv_exp_f32 v85, v85\n\t" \
    "v_exp_f32 v86, v86\n\tv_smfmac_f32_32x32x32_bf16 v[128:143], v[212:215], v[72:79], v224\n\tv_exp_f32 v87, v87\n\tv_exp_f32 v88, v88\n\t" \
    "v_exp_f32 v89, v89\n\tv_exp_f32 v90, v90\n\tv_smfmac_f32_32x32x32_bf16 v[144:159], v[208:211], v[80:87], v224\n\tv_exp_f32 v91, v91\n\t" \
    "v_exp_f32 v92, v92\n\tv_exp_f32 v93, v93\n\tv_exp_f32 v94, v94\n\tv_exp_f32 v95, v95\n\ts_waitcnt lgkmcnt(0)\n\t" \
    "v_mfma_f32_32x32x16_bf16 v[64:79], v[204:207], v[192:195], v[160:175]\n\tv_exp_f32 v96, v96\n\tv_exp_f32 v97, v97\n\tv_exp_f32 v98, v98\n\t" \
    "v_smfmac_f32_32x32x32_bf16 v[144:159], v[212:215], v[88:95], v224\n\tds_read_b128 v[208:211], %[va] offset:2688\n\t" \
    "ds_read_b128 v[212:215], %[va] offset:3360\n\tv_exp_f32 v99, v99\n\tv_exp_f32 v100, v100\n\tv_exp_f32 v101, v101\n\tv_exp_f32 v102, v102\n\t" \
    "v_mfma_f32_32x32x16_bf16 v[80:95], v[204:207], v[196:199], v[176:191]\n\tds_read_b128 v[204:207], %[ka] offset:3584\n\tv_exp_f32 v103, v103\n\t" \
    "v_exp_f32 v104, v104\n\tv_exp_f32 v105, v105\n\tv_exp_f32 v106, v106\n\tv_smfmac_f32_32x32x32_bf16 v[128:143], v[216:219], v[96:103], v224\n\t" \
    "global_load_lds_dwordx4 %[dv1], %[dg] offset:1024 sc1\n\tv_exp_f32 v107, v107\n\tv_exp_f32 v108, v108\n\tv_exp_f32 v109, v109\n\t" \
    "v_exp_f32 v110, v110\n\tv_mfma_f32_32x32x4_2b_bf16 v[64:95], v[202:203], v[200:201], v[64:95]\n\tds_read_b64 v[202:203], %[k2a] offset:2816\n\t" \
    "v_exp_f32 v111, v111\n\tv_exp_f32 v112, v112\n\tv_exp_f32 v113, v113\n\tv_exp_f32 v114, v114\n\tv_exp_f32 v115, v115\n\tv_exp_f32 v116, v116\n\t" \
    "v_exp_f32 v117, v117\n\tv_exp_f32 v118, v118\n\tv_smfmac_f32_32x32x32_bf16 v[128:143], v[220:223], v[104:111], v224\n\tv_exp_f32 v119, v119\n\t" \
    "v_exp_f32 v120, v120\n\tv_exp_f32 v121, v121\n\tv_exp_f32 v122, v122\n\tv_smfmac_f32_32x32x32_bf16 v[144:159], v[216:219], v[112:119], v224\n\t" \
    "v_exp_f32 v123, v123\n\tv_exp_f32 v124, v124\n\tv_exp_f32 v125, v125\n\tv_exp_f32 v126, v126\n\tv_exp_f32 v127, v127\n\t"
#define ASM_0B \
    "s_setprio 1\n\ts_waitcnt lgkmcnt(0)\n\tv_mfma_f32_32x32x16_bf16 v[96:111], v[204:207], v[192:195], v[160:175]\n\tv_exp_f32 v64, v64\n\t" \
    "v_exp_f32 v65, v65\n\tv_exp_f32 v66, v66\n\tv_smfmac_f32_32x32x32_bf16 v[144:159], v[220:223], v[120:127], v224\n\t" \
    "ds_read_b128 v[216:219], %[va] offset:4032\n\tds_read_b128 v[220:223], %[va] offset:4704\n\tv_exp_f32 v67, v67\n\tv_exp_f32 v68, v68\n\t" \
    "v_exp_f32 v69, v69\n\tv_exp_f32 v70, v70\n\tv_mfma_f32_32x32x16_bf16 v[112:127], v[204:207], v[196:199], v[176:191]\n\t" \
    "ds_read_b128 v[204:207], %[ka] offset:5120\n\tv_exp_f32 v71, v71\n\tv_exp_f32 v72, v72\n\tv_exp_f32 v73, v73\n\tv_exp_f32 v74, v74\n\t" \
    "v_smfmac_f32_32x32x32_bf16 v[128:143], v[208:211], v[64:71], v224\n\tglobal_load_lds_dwordx4 %[dv0], %[dg] offset:0 sc1\n\tv_exp_f32 v75, v75\n\t" \
    "v_exp_f32 v76, v76\n\tv_exp_f32 v77, v77\n\tv_exp_f32 v78, v78\n\tv_mfma_f32_32x32x4_2b_bf16 v[96:127], v[202:203], v[200:201], v[96:127]\n\t" \
    "ds_read_b64 v[202:203], %[k2a] offset:5120\n\tv_exp_f32 v79, v79\n\tv_exp_f32 v80, v80\n\tv_exp_f32 v81, v81\n\tv_exp_f32 v82, v82\n\t" \
    "v_exp_f32 v83, v83\n\tv_exp_f32 v84, v84\n\tv_exp_f32 v85, v85\n\tv_exp_f32 v86, v86\n\t" \
    "v_smfmac_f32_32x32x32_bf16 v[128:143], v[212:215], v[72:79], v224\n\tv_exp_f32 v87, v87\n\tv_exp_f32 v88, v88\n\tv_exp_f32 v89, v89\n\t" \
    "v_exp_f32 v90, v90\n\tv_smfmac_f32_32x32x32_bf16 v[144:159], v[208:211], v[80:87], v224\n\tv_exp_f32 v91, v91\n\tv_exp_f32 v92, v92\n\t" \
    "v_exp_f32 v93, v93\n\tv_exp_f32 v94, v94\n\tv_exp_f32 v95, v95\n\ts_waitcnt lgkmcnt(0)\n\t" \
    "v_mfma_f32_32x32x16_bf16 v[64:79], v[204:207], v[192:195], v[160:175]\n\tv_exp_f32 v96, v96\n\tv_exp_f32 v97, v97\n\tv_exp_f32 v98, v98\n\t" \
    "v_smfmac_f32_32x32x32_bf16 v[144:159], v[212:215], v[88:95], v224\n\tds_read_b128 v[208:211], %[va] offset:5376\n\t" \
    "ds_read_b128 v[212:215], %[va] offset:6048\n\tv_exp_f32 v99, v99\n\tv_exp_f32 v100, v100\n\tv_exp_f32 v101, v101\n\tv_exp_f32 v102, v102\n\t" \
    "v_mfma_f32_32x32x16_bf16 v[80:95], v[204:207], v[196:199], v[176:191]\n\tds_read_b128 v[204:207], %[ka] offset:6144\n\tv_exp_f32 v103, v103\n\t" \
    "v_exp_f32 v104, v104\n\tv_exp_f32 v105, v105\n\tv_exp_f32 v106, v106\n\tv_smfmac_f32_32x32x32_bf16 v[128:143], v[216:219], v[96:103], v224\n\t" \
    "global_load_lds_dwordx4 %[dv1], %[dg] offset:1024 sc1\n\tv_exp_f32 v107, v107\n\tv_exp_f32 v108, v108\n\tv_exp_f32 v109, v109\n\t" \
    "v_exp_f32 v110, v110\n\tv_mfma_f32_32x32x4_2b_bf16 v[64:95], v[202:203], v[200:201], v[64:95]\n\tds_read_b64 v[202:203], %[k2a] offset:5376\n\t" \
    "v_exp_f32 v111, v111\n\tv_exp_f32 v112, v112\n\tv_exp_f32 v113, v113\n\tv_exp_f32 v114, v114\n\tv_exp_f32 v115, v115\n\tv_exp_f32 v116, v116\n\t" \
    "v_exp_f32 v117, v117\n\tv_exp_f32 v118, v118\n\tv_smfmac_f32_32x32x32_bf16 v[128:143], v[220:223], v[104:111], v224\n\tv_exp_f32 v119, v119\n\t" \
    "v_exp_f32 v120, v120\n\tv_exp_f32 v121, v121\n\tv_exp_f32 v122, v122\n\tv_smfmac_f32_32x32x32_bf16 v[144:159], v[216:219], v[112:119], v224\n\t" \
    "v_exp_f32 v123, v123\n\tv_exp_f32 v124, v124\n\tv_exp_f32 v125, v125\n\tv_exp_f32 v126, v126\n\tv_exp_f32 v127, v127\n\ts_setprio 0\n\t" \
    "s_waitcnt lgkmcnt(0)\n\tv_mfma_f32_32x32x16_bf16 v[96:111], v[204:207], v[192:195], v[160:175]\n\tv_exp_f32 v64, v64\n\tv_exp_f32 v65, v65\n\t" \
    "v_exp_f32 v66, v66\n\tv_smfmac_f32_32x32x32_bf16 v[144:159], v[220:223], v[120:127], v224\n\tds_read_b128 v[216:219], %[va] offset:6720\n\t" \
    "ds_read_b128 v[220:223], %[va] offset:7392\n\tv_exp_f32 v67, v67\n\tv_exp_f32 v68, v68\n\tv_exp_f32 v69, v69\n\tv_exp_f32 v70, v70\n\t" \
    "v_mfma_f32_32x32x16_bf16 v[112:127], v[204:207], v[196:199], v[176:191]\n\tds_read_b128 v[204:207], %[ka] offset:7680\n\tv_exp_f32 v71, v71\n\t" \
    "v_exp_f32 v72, v72\n\tv_exp_f32 v73, v73\n\tv_exp_f32 v74, v74\n\tv_smfmac_f32_32x32x32_bf16 v[128:143], v[208:211], v[64:71], v224\n\t" \
    "global_load_lds_dwordx4 %[dv2], %[dg] offset:2048 sc1\n\tv_exp_f32 v75, v75\n\tv_exp_f32 v76, v76\n\tv_exp_f32 v77, v77\n\tv_exp_f32 v78, v78\n\t" \
    "v_mfma_f32_32x32x4_2b_bf16 v[96:127], v[202:203], v[200:201], v[96:127]\n\tds_read_b64 v[202:203], %[k2a] offset:7680\n\tv_exp_f32 v79, v79\n\t" \
    "v_exp_f32 v80, v80\n\tv_exp_f32 v81, v81\n\tv_exp_f32 v82, v82\n\tv_exp_f32 v83, v83\n\tv_exp_f32 v84, v84\n\tv_exp_f32 v85, v85\n\t" \
    "v_exp_f32 v86, v86\n\tv_smfmac_f32_32x32x32_bf16 v[128:143], v[212:215], v[72:79], v224\n\tv_exp_f32 v87, v87\n\tv_exp_f32 v88, v88\n\t" \
    "v_exp_f32 v89, v89\n\tv_exp_f32 v90, v90\n\tv_smfmac_f32_32x32x32_bf16 v[144:159], v[208:211], v[80:87], v224\n\tv_exp_f32 v91, v91\n\t" \
    "v_exp_f32 v92, v92\n\tv_exp_f32 v93, v93\n\tv_exp_f32 v94, v94\n\tv_exp_f32 v95, v95\n\ts_waitcnt lgkmcnt(0)\n\t" \
    "v_mfma_f32_32x32x16_bf16 v[64:79], v[204:207], v[192:195], v[160:175]\n\tv_exp_f32 v96, v96\n\tv_exp_f32 v97, v97\n\tv_exp_f32 v98, v98\n\t" \
    "v_smfmac_f32_32x32x32_bf16 v[144:159], v[212:215], v[88:95], v224\n\tds_read_b128 v[208:211], %[va] offset:8064\n\t" \
    "ds_read_b128 v[212:215], %[va] offset:8736\n\tv_exp_f32 v99, v99\n\tv_exp_f32 v100, v100\n\tv_exp_f32 v101, v101\n\tv_exp_f32 v102, v102\n\t" \
    "v_mfma_f32_32x32x16_bf16 v[80:95], v[204:207], v[196:199], v[176:191]\n\tds_read_b128 v[204:207], %[ka] offset:8704\n\tv_exp_f32 v103, v103\n\t" \
    "v_exp_f32 v104, v104\n\tv_exp_f32 v105, v105\n\tv_exp_f32 v106, v106\n\tv_smfmac_f32_32x32x32_bf16 v[128:143], v[216:219], v[96:103], v224\n\t" \
    "v_exp_f32 v107, v107\n\tv_exp_f32 v108, v108\n\tv_exp_f32 v109, v109\n\tv_exp_f32 v110, v110\n\t" \
    "v_mfma_f32_32x32x4_2b_bf16 v[64:95], v[202:203], v[200:201], v[64:95]\n\tds_read_b64 v[202:203], %[k2a] offset:7936\n\tv_exp_f32 v111, v111\n\t" \
    "v_exp_f32 v112, v112\n\tv_exp_f32 v113, v113\n\tv_exp_f32 v114, v114\n\tv_exp_f32 v115, v115\n\tv_exp_f32 v116, v116\n\tv_exp_f32 v117, v117\n\t" \
    "v_exp_f32 v118, v118\n\tv_smfmac_f32_32x32x32_bf16 v[128:143], v[220:223], v[104:111], v224\n\tv_exp_f32 v119, v119\n\tv_exp_f32 v120, v120\n\t" \
    "v_exp_f32 v121, v121\n\tv_exp_f32 v122, v122\n\tv_smfmac_f32_32x32x32_bf16 v[144:159], v[216:219], v[112:119], v224\n\tv_exp_f32 v123, v123\n\t" \
    "v_exp_f32 v124, v124\n\tv_exp_f32 v125, v125\n\tv_exp_f32 v126, v126\n\tv_exp_f32 v127, v127\n\t"
#define ASM_0C \
    "s_setprio 1\n\ts_waitcnt lgkmcnt(0)\n\tv_mfma_f32_32x32x16_bf16 v[96:111], v[204:207], v[192:195], v[160:175]\n\tv_exp_f32 v64, v64\n\t" \
    "v_exp_f32 v65, v65\n\tv_exp_f32 v66, v66\n\tv_smfmac_f32_32x32x32_bf16 v[144:159], v[220:223], v[120:127], v224\n\t" \
    "ds_read_b128 v[216:219], %[va] offset:4032\n\tds_read_b128 v[220:223], %[va] offset:4704\n\tv_exp_f32 v67, v67\n\tv_exp_f32 v68, v68\n\t" \
    "v_exp_f32 v69, v69\n\tv_exp_f32 v70, v70\n\tv_mfma_f32_32x32x16_bf16 v[112:127], v[204:207], v[196:199], v[176:191]\n\t" \
    "ds_read_b128 v[204:207], %[ka] offset:5120\n\tv_exp_f32 v71, v71\n\tv_exp_f32 v72, v72\n\tv_exp_f32 v73, v73\n\tv_exp_f32 v74, v74\n\t" \
    "v_smfmac_f32_32x32x32_bf16 v[128:143], v[208:211], v[64:71], v224\n\tglobal_load_lds_dwordx4 %[dv0], %[dg] offset:0 sc1\n\tv_exp_f32 v75, v75\n\t" \
    "v_exp_f32 v76, v76\n\tv_exp_f32 v77, v77\n\tv_exp_f32 v78, v78\n\tv_mfma_f32_32x32x4_2b_bf16 v[96:127], v[202:203], v[200:201], v[96:127]\n\t" \
    "ds_read_b64 v[202:203], %[k2a] offset:5120\n\tv_exp_f32 v79, v79\n\tv_exp_f32 v80, v80\n\tv_exp_f32 v81, v81\n\tv_exp_f32 v82, v82\n\t" \
    "v_exp_f32 v83, v83\n\tv_exp_f32 v84, v84\n\tv_exp_f32 v85, v85\n\tv_exp_f32 v86, v86\n\t" \
    "v_smfmac_f32_32x32x32_bf16 v[128:143], v[212:215], v[72:79], v224\n\tv_exp_f32 v87, v87\n\tv_exp_f32 v88, v88\n\tv_exp_f32 v89, v89\n\t" \
    "v_exp_f32 v90, v90\n\tv_smfmac_f32_32x32x32_bf16 v[144:159], v[208:211], v[80:87], v224\n\tv_exp_f32 v91, v91\n\tv_exp_f32 v92, v92\n\t" \
    "v_exp_f32 v93, v93\n\tv_exp_f32 v94, v94\n\tv_exp_f32 v95, v95\n\ts_waitcnt lgkmcnt(0)\n\t" \
    "v_mfma_f32_32x32x16_bf16 v[64:79], v[204:207], v[192:195], v[160:175]\n\tv_exp_f32 v96, v96\n\tv_exp_f32 v97, v97\n\tv_exp_f32 v98, v98\n\t" \
    "v_smfmac_f32_32x32x32_bf16 v[144:159], v[212:215], v[88:95], v224\n\tds_read_b128 v[208:211], %[va] offset:5376\n\t" \
    "ds_read_b128 v[212:215], %[va] offset:6048\n\tv_exp_f32 v99, v99\n\tv_exp_f32 v100, v100\n\tv_exp_f32 v101, v101\n\tv_exp_f32 v102, v102\n\t" \
    "v_mfma_f32_32x32x16_bf16 v[80:95], v[204:207], v[196:199], v[176:191]\n\tds_read_b128 v[204:207], %[ka] offset:6144\n\tv_exp_f32 v103, v103\n\t" \
    "v_exp_f32 v104, v104\n\tv_exp_f32 v105, v105\n\tv_exp_f32 v106, v106\n\tv_smfmac_f32_32x32x32_bf16 v[128:143], v[216:219], v[96:103], v224\n\t" \
    "global_load_lds_dwordx4 %[dv1], %[dg] offset:1024 sc1\n\tv_exp_f32 v107, v107\n\tv_exp_f32 v108, v108\n\tv_exp_f32 v109, v109\n\t" \
    "v_exp_f32 v110, v110\n\tv_mfma_f32_32x32x4_2b_bf16 v[64:95], v[202:203], v[200:201], v[64:95]\n\tds_read_b64 v[202:203], %[k2a] offset:5376\n\t" \
    "v_exp_f32 v111, v111\n\tv_exp_f32 v112, v112\n\tv_exp_f32 v113, v113\n\tv_exp_f32 v114, v114\n\tv_exp_f32 v115, v115\n\tv_exp_f32 v116, v116\n\t" \
    "v_exp_f32 v117, v117\n\tv_exp_f32 v118, v118\n\tv_smfmac_f32_32x32x32_bf16 v[128:143], v[220:223], v[104:111], v224\n\tv_exp_f32 v119, v119\n\t" \
    "v_exp_f32 v120, v120\n\tv_exp_f32 v121, v121\n\tv_exp_f32 v122, v122\n\tv_smfmac_f32_32x32x32_bf16 v[144:159], v[216:219], v[112:119], v224\n\t" \
    "v_exp_f32 v123, v123\n\tv_exp_f32 v124, v124\n\tv_exp_f32 v125, v125\n\tv_exp_f32 v126, v126\n\tv_exp_f32 v127, v127\n\ts_setprio 0\n\t" \
    "s_waitcnt lgkmcnt(0)\n\tv_mfma_f32_32x32x16_bf16 v[96:111], v[204:207], v[192:195], v[160:175]\n\tv_exp_f32 v64, v64\n\tv_exp_f32 v65, v65\n\t" \
    "v_exp_f32 v66, v66\n\tv_smfmac_f32_32x32x32_bf16 v[144:159], v[220:223], v[120:127], v224\n\tds_read_b128 v[216:219], %[va] offset:6720\n\t" \
    "ds_read_b128 v[220:223], %[va] offset:7392\n\tv_exp_f32 v67, v67\n\tv_exp_f32 v68, v68\n\tv_exp_f32 v69, v69\n\tv_exp_f32 v70, v70\n\t" \
    "v_mfma_f32_32x32x16_bf16 v[112:127], v[204:207], v[196:199], v[176:191]\n\tds_read_b128 v[204:207], %[ka] offset:7680\n\tv_exp_f32 v71, v71\n\t" \
    "v_exp_f32 v72, v72\n\tv_exp_f32 v73, v73\n\tv_exp_f32 v74, v74\n\tv_smfmac_f32_32x32x32_bf16 v[128:143], v[208:211], v[64:71], v224\n\t" \
    "global_load_lds_dwordx4 %[dv2], %[dg] offset:2048 sc1\n\tv_exp_f32 v75, v75\n\tv_exp_f32 v76, v76\n\tv_exp_f32 v77, v77\n\tv_exp_f32 v78, v78\n\t" \
    "v_mfma_f32_32x32x4_2b_bf16 v[96:127], v[202:203], v[200:201], v[96:127]\n\tds_read_b64 v[202:203], %[k2a] offset:7680\n\tv_exp_f32 v79, v79\n\t" \
    "v_exp_f32 v80, v80\n\tv_exp_f32 v81, v81\n\tv_exp_f32 v82, v82\n\tv_exp_f32 v83, v83\n\tv_exp_f32 v84, v84\n\tv_exp_f32 v85, v85\n\t" \
    "v_exp_f32 v86, v86\n\tv_smfmac_f32_32x32x32_bf16 v[128:143], v[212:215], v[72:79], v224\n\tv_exp_f32 v87, v87\n\tv_exp_f32 v88, v88\n\t" \
    "v_exp_f32 v89, v89\n\tv_exp_f32 v90, v90\n\tv_smfmac_f32_32x32x32_bf16 v[144:159], v[208:211], v[80:87], v224\n\tv_exp_f32 v91, v91\n\t" \
    "v_exp_f32 v92, v92\n\tv_exp_f32 v93, v93\n\tv_exp_f32 v94, v94\n\tv_exp_f32 v95, v95\n\t"
#define ASM_0D \
    "s_setprio 0\n\ts_waitcnt lgkmcnt(0)\n\tv_mfma_f32_32x32x16_bf16 v[64:79], v[204:207], v[192:195], v[160:175]\n\tv_exp_f32 v96, v96\n\t" \
    "v_exp_f32 v97, v97\n\tv_exp_f32 v98, v98\n\tv_smfmac_f32_32x32x32_bf16 v[144:159], v[212:215], v[88:95], v224\n\t" \
    "ds_read_b128 v[208:211], %[va] offset:8064\n\tds_read_b128 v[212:215], %[va] offset:8736\n\tv_exp_f32 v99, v99\n\tv_exp_f32 v100, v100\n\t" \
    "v_exp_f32 v101, v101\n\tv_exp_f32 v102, v102\n\tv_mfma_f32_32x32x16_bf16 v[80:95], v[204:207], v[196:199], v[176:191]\n\t" \
    "ds_read_b128 v[204:207], %[ka] offset:8704\n\tv_exp_f32 v103, v103\n\tv_exp_f32 v104, v104\n\tv_exp_f32 v105, v105\n\tv_exp_f32 v106, v106\n\t" \
    "v_smfmac_f32_32x32x32_bf16 v[128:143], v[216:219], v[96:103], v224\n\tv_exp_f32 v107, v107\n\tv_exp_f32 v108, v108\n\tv_exp_f32 v109, v109\n\t" \
    "v_exp_f32 v110, v110\n\tv_mfma_f32_32x32x4_2b_bf16 v[64:95], v[202:203], v[200:201], v[64:95]\n\tds_read_b64 v[202:203], %[k2a] offset:7936\n\t" \
    "v_exp_f32 v111, v111\n\tv_exp_f32 v112, v112\n\tv_exp_f32 v113, v113\n\tv_exp_f32 v114, v114\n\tv_exp_f32 v115, v115\n\tv_exp_f32 v116, v116\n\t" \
    "v_exp_f32 v117, v117\n\tv_exp_f32 v118, v118\n\tv_smfmac_f32_32x32x32_bf16 v[128:143], v[220:223], v[104:111], v224\n\tv_exp_f32 v119, v119\n\t" \
    "v_exp_f32 v120, v120\n\tv_exp_f32 v121, v121\n\tv_exp_f32 v122, v122\n\tv_smfmac_f32_32x32x32_bf16 v[144:159], v[216:219], v[112:119], v224\n\t" \
    "v_exp_f32 v123, v123\n\tv_exp_f32 v124, v124\n\tv_exp_f32 v125, v125\n\tv_exp_f32 v126, v126\n\tv_exp_f32 v127, v127\n\t"
#define ASM_1HC \
    "s_setprio 2\n\ts_waitcnt lgkmcnt(0)\n\tv_mfma_f32_32x32x16_bf16 v[96:111], v[204:207], v[192:195], v[160:175]\n\tv_exp_f32 v64, v64\n\t" \
    "v_exp_f32 v65, v65\n\tv_exp_f32 v66, v66\n\tv_smfmac_f32_32x32x32_bf16 v[144:159], v[220:223], v[120:127], v224\n\t" \
    "ds_read_b128 v[216:219], %[vp] offset:9408\n\tds_read_b128 v[220:223], %[vp] offset:10080\n\tv_exp_f32 v67, v67\n\tv_exp_f32 v68, v68\n\t" \
    "v_exp_f32 v69, v69\n\tv_exp_f32 v70, v70\n\tv_mfma_f32_32x32x16_bf16 v[112:127], v[204:207], v[196:199], v[176:191]\n\t" \
    "ds_read_b128 v[204:207], %[ka] offset:0\n\tv_exp_f32 v71, v71\n\tv_exp_f32 v72, v72\n\tv_exp_f32 v73, v73\n\tv_exp_f32 v74, v74\n\t" \
    "v_smfmac_f32_32x32x32_bf16 v[128:143], v[208:211], v[64:71], v224\n\tglobal_load_lds_dwordx4 %[dv0], %[dg] offset:0 sc1\n\tv_exp_f32 v75, v75\n\t" \
    "v_exp_f32 v76, v76\n\tv_exp_f32 v77, v77\n\tv_exp_f32 v78, v78\n\tv_mfma_f32_32x32x4_2b_bf16 v[96:127], v[202:203], v[200:201], v[96:127]\n\t" \
    "ds_read_b64 v[202:203], %[k2a] offset:0\n\tv_exp_f32 v79, v79\n\tv_exp_f32 v80, v80\n\tv_exp_f32 v81, v81\n\tv_exp_f32 v82, v82\n\t" \
    "v_exp_f32 v83, v83\n\tv_exp_f32 v84, v84\n\tv_exp_f32 v85, v85\n\tv_exp_f32 v86, v86\n\t" \
    "v_smfmac_f32_32x32x32_bf16 v[128:143], v[212:215], v[72:79], v224\n\tglobal_store_dwordx4 %[oo], %[em], %[o1] sc1\n\tv_exp_f32 v87, v87\n\t" \
    "v_exp_f32 v88, v88\n\tv_exp_f32 v89, v89\n\tv_exp_f32 v90, v90\n\tv_smfmac_f32_32x32x32_bf16 v[144:159], v[208:211], v[80:87], v224\n\t" \
    "global_store_dwordx4 %[oo], %[em], %[o1] offset:2048 sc1\n\tv_exp_f32 v91, v91\n\tv_exp_f32 v92, v92\n\tv_exp_f32 v93, v93\n\tv_exp_f32 v94, v94\n\t" \
    "v_exp_f32 v95, v95\n\ts_waitcnt lgkmcnt(0)\n\tv_mfma_f32_32x32x16_bf16 v[64:79], v[204:207], v[192:195], v[160:175]\n\tv_exp_f32 v96, v96\n\t" \
    "v_exp_f32 v97, v97\n\tv_exp_f32 v98, v98\n\tv_smfmac_f32_32x32x32_bf16 v[144:159], v[212:215], v[88:95], v224\n\t" \
    "ds_read_b128 v[208:211], %[va] offset:0\n\tds_read_b128 v[212:215], %[va] offset:672\n\tv_exp_f32 v99, v99\n\tv_exp_f32 v100, v100\n\t" \
    "v_exp_f32 v101, v101\n\tv_exp_f32 v102, v102\n\tv_mfma_f32_32x32x16_bf16 v[80:95], v[204:207], v[196:199], v[176:191]\n\t" \
    "ds_read_b128 v[204:207], %[ka] offset:1024\n\tv_exp_f32 v103, v103\n\tv_exp_f32 v104, v104\n\tv_exp_f32 v105, v105\n\tv_exp_f32 v106, v106\n\t" \
    "v_smfmac_f32_32x32x32_bf16 v[128:143], v[216:219], v[96:103], v224\n\tglobal_load_lds_dwordx4 %[dv1], %[dg] offset:1024 sc1\n\t" \
    "v_exp_f32 v107, v107\n\tv_exp_f32 v108, v108\n\tv_exp_f32 v109, v109\n\tv_exp_f32 v110, v110\n\t" \
    "v_mfma_f32_32x32x4_2b_bf16 v[64:95], v[202:203], v[200:201], v[64:95]\n\tds_read_b64 v[202:203], %[k2a] offset:256\n\tv_exp_f32 v111, v111\n\t" \
    "v_exp_f32 v112, v112\n\tv_exp_f32 v113, v113\n\tv_exp_f32 v114, v114\n\tv_exp_f32 v115, v115\n\tv_exp_f32 v116, v116\n\tv_exp_f32 v117, v117\n\t" \
    "v_exp_f32 v118, v118\n\tv_smfmac_f32_32x32x32_bf16 v[128:143], v[220:223], v[104:111], v224\n\tv_exp_f32 v119, v119\n\tv_exp_f32 v120, v120\n\t" \
    "v_exp_f32 v121, v121\n\tv_exp_f32 v122, v122\n\tv_smfmac_f32_32x32x32_bf16 v[144:159], v[216:219], v[112:119], v224\n\ts_mov_b64 exec, 0xffffffff\n\t" \
    "global_store_dwordx4 %[oo], %[em], %[o2] sc1\n\ts_mov_b64 exec, -1\n\tv_exp_f32 v123, v123\n\tv_exp_f32 v124, v124\n\tv_exp_f32 v125, v125\n\t" \
    "v_exp_f32 v126, v126\n\tv_exp_f32 v127, v127\n\t"
#define ASM_1AC \
    "s_setprio 2\n\ts_waitcnt lgkmcnt(0)\n\tv_mfma_f32_32x32x16_bf16 v[96:111], v[204:207], v[192:195], v[160:175]\n\tv_exp_f32 v64, v64\n\t" \
    "v_exp_f32 v65, v65\n\tv_exp_f32 v66, v66\n\tv_smfmac_f32_32x32x32_bf16 v[144:159], v[220:223], v[120:127], v224\n\t" \
    "ds_read_b128 v[216:219], %[va] offset:1344\n\tds_read_b128 v[220:223], %[va] offset:2016\n\tv_exp_f32 v67, v67\n\tv_exp_f32 v68, v68\n\t" \
    "v_exp_f32 v69, v69\n\tv_exp_f32 v70, v70\n\tv_mfma_f32_32x32x16_bf16 v[112:127], v[204:207], v[196:199], v[176:191]\n\t" \
    "ds_read_b128 v[204:207], %[ka] offset:2560\n\tv_exp_f32 v71, v71\n\tv_exp_f32 v72, v72\n\tv_exp_f32 v73, v73\n\tv_exp_f32 v74, v74\n\t" \
    "v_smfmac_f32_32x32x32_bf16 v[128:143], v[208:211], v[64:71], v224\n\tglobal_load_lds_dwordx4 %[dv0], %[dg] offset:0 sc1\n\tv_exp_f32 v75, v75\n\t" \
    "v_exp_f32 v76, v76\n\tv_exp_f32 v77, v77\n\tv_exp_f32 v78, v78\n\tv_mfma_f32_32x32x4_2b_bf16 v[96:127], v[202:203], v[200:201], v[96:127]\n\t" \
    "ds_read_b64 v[202:203], %[k2a] offset:2560\n\tv_exp_f32 v79, v79\n\tv_exp_f32 v80, v80\n\tv_exp_f32 v81, v81\n\tv_exp_f32 v82, v82\n\t" \
    "v_exp_f32 v83, v83\n\tv_exp_f32 v84, v84\n\tv_exp_f32 v85, v85\n\tv_exp_f32 v86, v86\n\t" \
    "v_smfmac_f32_32x32x32_bf16 v[128:143], v[212:215], v[72:79], v224\n\tglobal_store_dwordx4 %[oo], %[em], %[o1] sc1\n\tv_exp_f32 v87, v87\n\t" \
    "v_exp_f32 v88, v88\n\tv_exp_f32 v89, v89\n\tv_exp_f32 v90, v90\n\tv_smfmac_f32_32x32x32_bf16 v[144:159], v[208:211], v[80:87], v224\n\t" \
    "global_store_dwordx4 %[oo], %[em], %[o1] offset:2048 sc1\n\tv_exp_f32 v91, v91\n\tv_exp_f32 v92, v92\n\tv_exp_f32 v93, v93\n\tv_exp_f32 v94, v94\n\t" \
    "v_exp_f32 v95, v95\n\ts_setprio 1\n\ts_waitcnt lgkmcnt(0)\n\tv_mfma_f32_32x32x16_bf16 v[64:79], v[204:207], v[192:195], v[160:175]\n\t" \
    "v_exp_f32 v96, v96\n\tv_exp_f32 v97, v97\n\tv_exp_f32 v98, v98\n\tv_smfmac_f32_32x32x32_bf16 v[144:159], v[212:215], v[88:95], v224\n\t" \
    "ds_read_b128 v[208:211], %[va] offset:2688\n\tds_read_b128 v[212:215], %[va] offset:3360\n\tv_exp_f32 v99, v99\n\tv_exp_f32 v100, v100\n\t" \
    "v_exp_f32 v101, v101\n\tv_exp_f32 v102, v102\n\tv_mfma_f32_32x32x16_bf16 v[80:95], v[204:207], v[196:199], v[176:191]\n\t" \
    "ds_read_b128 v[204:207], %[ka] offset:3584\n\tv_exp_f32 v103, v103\n\tv_exp_f32 v104, v104\n\tv_exp_f32 v105, v105\n\tv_exp_f32 v106, v106\n\t" \
    "v_smfmac_f32_32x32x32_bf16 v[128:143], v[216:219], v[96:103], v224\n\tglobal_load_lds_dwordx4 %[dv1], %[dg] offset:1024 sc1\n\t" \
    "v_exp_f32 v107, v107\n\tv_exp_f32 v108, v108\n\tv_exp_f32 v109, v109\n\tv_exp_f32 v110, v110\n\t" \
    "v_mfma_f32_32x32x4_2b_bf16 v[64:95], v[202:203], v[200:201], v[64:95]\n\tds_read_b64 v[202:203], %[k2a] offset:2816\n\tv_exp_f32 v111, v111\n\t" \
    "v_exp_f32 v112, v112\n\tv_exp_f32 v113, v113\n\tv_exp_f32 v114, v114\n\tv_exp_f32 v115, v115\n\tv_exp_f32 v116, v116\n\tv_exp_f32 v117, v117\n\t" \
    "v_exp_f32 v118, v118\n\tv_smfmac_f32_32x32x32_bf16 v[128:143], v[220:223], v[104:111], v224\n\tv_exp_f32 v119, v119\n\tv_exp_f32 v120, v120\n\t" \
    "v_exp_f32 v121, v121\n\tv_exp_f32 v122, v122\n\tv_smfmac_f32_32x32x32_bf16 v[144:159], v[216:219], v[112:119], v224\n\ts_mov_b64 exec, 0xffffffff\n\t" \
    "global_store_dwordx4 %[oo], %[em], %[o2] sc1\n\ts_mov_b64 exec, -1\n\tv_exp_f32 v123, v123\n\tv_exp_f32 v124, v124\n\tv_exp_f32 v125, v125\n\t" \
    "v_exp_f32 v126, v126\n\tv_exp_f32 v127, v127\n\t"
#define ASM_1H \
    "s_setprio 2\n\ts_waitcnt lgkmcnt(0)\n\tv_mfma_f32_32x32x16_bf16 v[96:111], v[204:207], v[192:195], v[160:175]\n\tv_exp_f32 v64, v64\n\t" \
    "v_exp_f32 v65, v65\n\tv_exp_f32 v66, v66\n\tv_smfmac_f32_32x32x32_bf16 v[144:159], v[220:223], v[120:127], v224\n\t" \
    "ds_read_b128 v[216:219], %[vp] offset:9408\n\tds_read_b128 v[220:223], %[vp] offset:10080\n\tv_exp_f32 v67, v67\n\tv_exp_f32 v68, v68\n\t" \
    "v_exp_f32 v69, v69\n\tv_exp_f32 v70, v70\n\tv_mfma_f32_32x32x16_bf16 v[112:127], v[204:207], v[196:199], v[176:191]\n\t" \
    "ds_read_b128 v[204:207], %[ka] offset:0\n\tv_exp_f32 v71, v71\n\tv_exp_f32 v72, v72\n\tv_exp_f32 v73, v73\n\tv_exp_f32 v74, v74\n\t" \
    "v_smfmac_f32_32x32x32_bf16 v[128:143], v[208:211], v[64:71], v224\n\tglobal_load_lds_dwordx4 %[dv0], %[dg] offset:0 sc1\n\tv_exp_f32 v75, v75\n\t" \
    "v_exp_f32 v76, v76\n\tv_exp_f32 v77, v77\n\tv_exp_f32 v78, v78\n\tv_mfma_f32_32x32x4_2b_bf16 v[96:127], v[202:203], v[200:201], v[96:127]\n\t" \
    "ds_read_b64 v[202:203], %[k2a] offset:0\n\tv_exp_f32 v79, v79\n\tv_exp_f32 v80, v80\n\tv_exp_f32 v81, v81\n\tv_exp_f32 v82, v82\n\t" \
    "v_exp_f32 v83, v83\n\tv_exp_f32 v84, v84\n\tv_exp_f32 v85, v85\n\tv_exp_f32 v86, v86\n\t" \
    "v_smfmac_f32_32x32x32_bf16 v[128:143], v[212:215], v[72:79], v224\n\tv_exp_f32 v87, v87\n\tv_exp_f32 v88, v88\n\tv_exp_f32 v89, v89\n\t" \
    "v_exp_f32 v90, v90\n\tv_smfmac_f32_32x32x32_bf16 v[144:159], v[208:211], v[80:87], v224\n\tv_exp_f32 v91, v91\n\tv_exp_f32 v92, v92\n\t" \
    "v_exp_f32 v93, v93\n\tv_exp_f32 v94, v94\n\tv_exp_f32 v95, v95\n\ts_waitcnt lgkmcnt(0)\n\t" \
    "v_mfma_f32_32x32x16_bf16 v[64:79], v[204:207], v[192:195], v[160:175]\n\tv_exp_f32 v96, v96\n\tv_exp_f32 v97, v97\n\tv_exp_f32 v98, v98\n\t" \
    "v_smfmac_f32_32x32x32_bf16 v[144:159], v[212:215], v[88:95], v224\n\tds_read_b128 v[208:211], %[va] offset:0\n\t" \
    "ds_read_b128 v[212:215], %[va] offset:672\n\tv_exp_f32 v99, v99\n\tv_exp_f32 v100, v100\n\tv_exp_f32 v101, v101\n\tv_exp_f32 v102, v102\n\t" \
    "v_mfma_f32_32x32x16_bf16 v[80:95], v[204:207], v[196:199], v[176:191]\n\tds_read_b128 v[204:207], %[ka] offset:1024\n\tv_exp_f32 v103, v103\n\t" \
    "v_exp_f32 v104, v104\n\tv_exp_f32 v105, v105\n\tv_exp_f32 v106, v106\n\tv_smfmac_f32_32x32x32_bf16 v[128:143], v[216:219], v[96:103], v224\n\t" \
    "global_load_lds_dwordx4 %[dv1], %[dg] offset:1024 sc1\n\tv_exp_f32 v107, v107\n\tv_exp_f32 v108, v108\n\tv_exp_f32 v109, v109\n\t" \
    "v_exp_f32 v110, v110\n\tv_mfma_f32_32x32x4_2b_bf16 v[64:95], v[202:203], v[200:201], v[64:95]\n\tds_read_b64 v[202:203], %[k2a] offset:256\n\t" \
    "v_exp_f32 v111, v111\n\tv_exp_f32 v112, v112\n\tv_exp_f32 v113, v113\n\tv_exp_f32 v114, v114\n\tv_exp_f32 v115, v115\n\tv_exp_f32 v116, v116\n\t" \
    "v_exp_f32 v117, v117\n\tv_exp_f32 v118, v118\n\tv_smfmac_f32_32x32x32_bf16 v[128:143], v[220:223], v[104:111], v224\n\tv_exp_f32 v119, v119\n\t" \
    "v_exp_f32 v120, v120\n\tv_exp_f32 v121, v121\n\tv_exp_f32 v122, v122\n\tv_smfmac_f32_32x32x32_bf16 v[144:159], v[216:219], v[112:119], v224\n\t" \
    "v_exp_f32 v123, v123\n\tv_exp_f32 v124, v124\n\tv_exp_f32 v125, v125\n\tv_exp_f32 v126, v126\n\tv_exp_f32 v127, v127\n\t"
#define ASM_1A \
    "s_setprio 2\n\ts_waitcnt lgkmcnt(0)\n\tv_mfma_f32_32x32x16_bf16 v[96:111], v[204:207], v[192:195], v[160:175]\n\tv_exp_f32 v64, v64\n\t" \
    "v_exp_f32 v65, v65\n\tv_exp_f32 v66, v66\n\tv_smfmac_f32_32x32x32_bf16 v[144:159], v[220:223], v[120:127], v224\n\t" \
    "ds_read_b128 v[216:219], %[va] offset:1344\n\tds_read_b128 v[220:223], %[va] offset:2016\n\tv_exp_f32 v67, v67\n\tv_exp_f32 v68, v68\n\t" \
    "v_exp_f32 v69, v69\n\tv_exp_f32 v70, v70\n\tv_mfma_f32_32x32x16_bf16 v[112:127], v[204:207], v[196:199], v[176:191]\n\t" \
    "ds_read_b128 v[204:207], %[ka] offset:2560\n\tv_exp_f32 v71, v71\n\tv_exp_f32 v72, v72\n\tv_exp_f32 v73, v73\n\tv_exp_f32 v74, v74\n\t" \
    "v_smfmac_f32_32x32x32_bf16 v[128:143], v[208:211], v[64:71], v224\n\tglobal_load_lds_dwordx4 %[dv0], %[dg] offset:0 sc1\n\tv_exp_f32 v75, v75\n\t" \
    "v_exp_f32 v76, v76\n\tv_exp_f32 v77, v77\n\tv_exp_f32 v78, v78\n\tv_mfma_f32_32x32x4_2b_bf16 v[96:127], v[202:203], v[200:201], v[96:127]\n\t" \
    "ds_read_b64 v[202:203], %[k2a] offset:2560\n\tv_exp_f32 v79, v79\n\tv_exp_f32 v80, v80\n\tv_exp_f32 v81, v81\n\tv_exp_f32 v82, v82\n\t" \
    "v_exp_f32 v83, v83\n\tv_exp_f32 v84, v84\n\tv_exp_f32 v85, v85\n\tv_exp_f32 v86, v86\n\t" \
    "v_smfmac_f32_32x32x32_bf16 v[128:143], v[212:215], v[72:79], v224\n\tv_exp_f32 v87, v87\n\tv_exp_f32 v88, v88\n\tv_exp_f32 v89, v89\n\t" \
    "v_exp_f32 v90, v90\n\tv_smfmac_f32_32x32x32_bf16 v[144:159], v[208:211], v[80:87], v224\n\tv_exp_f32 v91, v91\n\tv_exp_f32 v92, v92\n\t" \
    "v_exp_f32 v93, v93\n\tv_exp_f32 v94, v94\n\tv_exp_f32 v95, v95\n\ts_setprio 1\n\ts_waitcnt lgkmcnt(0)\n\t" \
    "v_mfma_f32_32x32x16_bf16 v[64:79], v[204:207], v[192:195], v[160:175]\n\tv_exp_f32 v96, v96\n\tv_exp_f32 v97, v97\n\tv_exp_f32 v98, v98\n\t" \
    "v_smfmac_f32_32x32x32_bf16 v[144:159], v[212:215], v[88:95], v224\n\tds_read_b128 v[208:211], %[va] offset:2688\n\t" \
    "ds_read_b128 v[212:215], %[va] offset:3360\n\tv_exp_f32 v99, v99\n\tv_exp_f32 v100, v100\n\tv_exp_f32 v101, v101\n\tv_exp_f32 v102, v102\n\t" \
    "v_mfma_f32_32x32x16_bf16 v[80:95], v[204:207], v[196:199], v[176:191]\n\tds_read_b128 v[204:207], %[ka] offset:3584\n\tv_exp_f32 v103, v103\n\t" \
    "v_exp_f32 v104, v104\n\tv_exp_f32 v105, v105\n\tv_exp_f32 v106, v106\n\tv_smfmac_f32_32x32x32_bf16 v[128:143], v[216:219], v[96:103], v224\n\t" \
    "global_load_lds_dwordx4 %[dv1], %[dg] offset:1024 sc1\n\tv_exp_f32 v107, v107\n\tv_exp_f32 v108, v108\n\tv_exp_f32 v109, v109\n\t" \
    "v_exp_f32 v110, v110\n\tv_mfma_f32_32x32x4_2b_bf16 v[64:95], v[202:203], v[200:201], v[64:95]\n\tds_read_b64 v[202:203], %[k2a] offset:2816\n\t" \
    "v_exp_f32 v111, v111\n\tv_exp_f32 v112, v112\n\tv_exp_f32 v113, v113\n\tv_exp_f32 v114, v114\n\tv_exp_f32 v115, v115\n\tv_exp_f32 v116, v116\n\t" \
    "v_exp_f32 v117, v117\n\tv_exp_f32 v118, v118\n\tv_smfmac_f32_32x32x32_bf16 v[128:143], v[220:223], v[104:111], v224\n\tv_exp_f32 v119, v119\n\t" \
    "v_exp_f32 v120, v120\n\tv_exp_f32 v121, v121\n\tv_exp_f32 v122, v122\n\tv_smfmac_f32_32x32x32_bf16 v[144:159], v[216:219], v[112:119], v224\n\t" \
    "v_exp_f32 v123, v123\n\tv_exp_f32 v124, v124\n\tv_exp_f32 v125, v125\n\tv_exp_f32 v126, v126\n\tv_exp_f32 v127, v127\n\t"
#define ASM_1B \
    "s_setprio 1\n\ts_waitcnt lgkmcnt(0)\n\tv_mfma_f32_32x32x16_bf16 v[96:111], v[204:207], v[192:195], v[160:175]\n\tv_exp_f32 v64, v64\n\t" \
    "v_exp_f32 v65, v65\n\tv_exp_f32 v66, v66\n\tv_smfmac_f32_32x32x32_bf16 v[144:159], v[220:223], v[120:127], v224\n\t" \
    "ds_read_b128 v[216:219], %[va] offset:4032\n\tds_read_b128 v[220:223], %[va] offset:4704\n\tv_exp_f32 v67, v67\n\tv_exp_f32 v68, v68\n\t" \
    "v_exp_f32 v69, v69\n\tv_exp_f32 v70, v70\n\tv_mfma_f32_32x32x16_bf16 v[112:127], v[204:207], v[196:199], v[176:191]\n\t" \
    "ds_read_b128 v[204:207], %[ka] offset:5120\n\tv_exp_f32 v71, v71\n\tv_exp_f32 v72, v72\n\tv_exp_f32 v73, v73\n\tv_exp_f32 v74, v74\n\t" \
    "v_smfmac_f32_32x32x32_bf16 v[128:143], v[208:211], v[64:71], v224\n\tglobal_load_lds_dwordx4 %[dv0], %[dg] offset:0 sc1\n\tv_exp_f32 v75, v75\n\t" \
    "v_exp_f32 v76, v76\n\tv_exp_f32 v77, v77\n\tv_exp_f32 v78, v78\n\tv_mfma_f32_32x32x4_2b_bf16 v[96:127], v[202:203], v[200:201], v[96:127]\n\t" \
    "ds_read_b64 v[202:203], %[k2a] offset:5120\n\tv_exp_f32 v79, v79\n\tv_exp_f32 v80, v80\n\tv_exp_f32 v81, v81\n\tv_exp_f32 v82, v82\n\t" \
    "v_exp_f32 v83, v83\n\tv_exp_f32 v84, v84\n\tv_exp_f32 v85, v85\n\tv_exp_f32 v86, v86\n\t" \
    "v_smfmac_f32_32x32x32_bf16 v[128:143], v[212:215], v[72:79], v224\n\tv_exp_f32 v87, v87\n\tv_exp_f32 v88, v88\n\tv_exp_f32 v89, v89\n\t" \
    "v_exp_f32 v90, v90\n\tv_smfmac_f32_32x32x32_bf16 v[144:159], v[208:211], v[80:87], v224\n\tv_exp_f32 v91, v91\n\tv_exp_f32 v92, v92\n\t" \
    "v_exp_f32 v93, v93\n\tv_exp_f32 v94, v94\n\tv_exp_f32 v95, v95\n\ts_waitcnt lgkmcnt(0)\n\t" \
    "v_mfma_f32_32x32x16_bf16 v[64:79], v[204:207], v[192:195], v[160:175]\n\tv_exp_f32 v96, v96\n\tv_exp_f32 v97, v97\n\tv_exp_f32 v98, v98\n\t" \
    "v_smfmac_f32_32x32x32_bf16 v[144:159], v[212:215], v[88:95], v224\n\tds_read_b128 v[208:211], %[va] offset:5376\n\t" \
    "ds_read_b128 v[212:215], %[va] offset:6048\n\tv_exp_f32 v99, v99\n\tv_exp_f32 v100, v100\n\tv_exp_f32 v101, v101\n\tv_exp_f32 v102, v102\n\t" \
    "v_mfma_f32_32x32x16_bf16 v[80:95], v[204:207], v[196:199], v[176:191]\n\tds_read_b128 v[204:207], %[ka] offset:6144\n\tv_exp_f32 v103, v103\n\t" \
    "v_exp_f32 v104, v104\n\tv_exp_f32 v105, v105\n\tv_exp_f32 v106, v106\n\tv_smfmac_f32_32x32x32_bf16 v[128:143], v[216:219], v[96:103], v224\n\t" \
    "v_exp_f32 v107, v107\n\tv_exp_f32 v108, v108\n\tv_exp_f32 v109, v109\n\tv_exp_f32 v110, v110\n\t" \
    "v_mfma_f32_32x32x4_2b_bf16 v[64:95], v[202:203], v[200:201], v[64:95]\n\tds_read_b64 v[202:203], %[k2a] offset:5376\n\tv_exp_f32 v111, v111\n\t" \
    "v_exp_f32 v112, v112\n\tv_exp_f32 v113, v113\n\tv_exp_f32 v114, v114\n\tv_exp_f32 v115, v115\n\tv_exp_f32 v116, v116\n\tv_exp_f32 v117, v117\n\t" \
    "v_exp_f32 v118, v118\n\tv_smfmac_f32_32x32x32_bf16 v[128:143], v[220:223], v[104:111], v224\n\tv_exp_f32 v119, v119\n\tv_exp_f32 v120, v120\n\t" \
    "v_exp_f32 v121, v121\n\tv_exp_f32 v122, v122\n\tv_smfmac_f32_32x32x32_bf16 v[144:159], v[216:219], v[112:119], v224\n\tv_exp_f32 v123, v123\n\t" \
    "v_exp_f32 v124, v124\n\tv_exp_f32 v125, v125\n\tv_exp_f32 v126, v126\n\tv_exp_f32 v127, v127\n\ts_setprio 0\n\ts_waitcnt lgkmcnt(0)\n\t" \
    "v_mfma_f32_32x32x16_bf16 v[96:111], v[204:207], v[192:195], v[160:175]\n\tv_exp_f32 v64, v64\n\tv_exp_f32 v65, v65\n\tv_exp_f32 v66, v66\n\t" \
    "v_smfmac_f32_32x32x32_bf16 v[144:159], v[220:223], v[120:127], v224\n\tds_read_b128 v[216:219], %[va] offset:6720\n\t" \
    "ds_read_b128 v[220:223], %[va] offset:7392\n\tv_exp_f32 v67, v67\n\tv_exp_f32 v68, v68\n\tv_exp_f32 v69, v69\n\tv_exp_f32 v70, v70\n\t" \
    "v_mfma_f32_32x32x16_bf16 v[112:127], v[204:207], v[196:199], v[176:191]\n\tds_read_b128 v[204:207], %[ka] offset:7680\n\tv_exp_f32 v71, v71\n\t" \
    "v_exp_f32 v72, v72\n\tv_exp_f32 v73, v73\n\tv_exp_f32 v74, v74\n\tv_smfmac_f32_32x32x32_bf16 v[128:143], v[208:211], v[64:71], v224\n\t" \
    "v_exp_f32 v75, v75\n\tv_exp_f32 v76, v76\n\tv_exp_f32 v77, v77\n\tv_exp_f32 v78, v78\n\t" \
    "v_mfma_f32_32x32x4_2b_bf16 v[96:127], v[202:203], v[200:201], v[96:127]\n\tds_read_b64 v[202:203], %[k2a] offset:7680\n\tv_exp_f32 v79, v79\n\t" \
    "v_exp_f32 v80, v80\n\tv_exp_f32 v81, v81\n\tv_exp_f32 v82, v82\n\tv_exp_f32 v83, v83\n\tv_exp_f32 v84, v84\n\tv_exp_f32 v85, v85\n\t" \
    "v_exp_f32 v86, v86\n\tv_smfmac_f32_32x32x32_bf16 v[128:143], v[212:215], v[72:79], v224\n\tv_exp_f32 v87, v87\n\tv_exp_f32 v88, v88\n\t" \
    "v_exp_f32 v89, v89\n\tv_exp_f32 v90, v90\n\tv_smfmac_f32_32x32x32_bf16 v[144:159], v[208:211], v[80:87], v224\n\tv_exp_f32 v91, v91\n\t" \
    "v_exp_f32 v92, v92\n\tv_exp_f32 v93, v93\n\tv_exp_f32 v94, v94\n\tv_exp_f32 v95, v95\n\ts_waitcnt lgkmcnt(0)\n\t" \
    "v_mfma_f32_32x32x16_bf16 v[64:79], v[204:207], v[192:195], v[160:175]\n\tv_exp_f32 v96, v96\n\tv_exp_f32 v97, v97\n\tv_exp_f32 v98, v98\n\t" \
    "v_smfmac_f32_32x32x32_bf16 v[144:159], v[212:215], v[88:95], v224\n\tds_read_b128 v[208:211], %[va] offset:8064\n\t" \
    "ds_read_b128 v[212:215], %[va] offset:8736\n\tv_exp_f32 v99, v99\n\tv_exp_f32 v100, v100\n\tv_exp_f32 v101, v101\n\tv_exp_f32 v102, v102\n\t" \
    "v_mfma_f32_32x32x16_bf16 v[80:95], v[204:207], v[196:199], v[176:191]\n\tds_read_b128 v[204:207], %[ka] offset:8704\n\tv_exp_f32 v103, v103\n\t" \
    "v_exp_f32 v104, v104\n\tv_exp_f32 v105, v105\n\tv_exp_f32 v106, v106\n\tv_smfmac_f32_32x32x32_bf16 v[128:143], v[216:219], v[96:103], v224\n\t" \
    "v_exp_f32 v107, v107\n\tv_exp_f32 v108, v108\n\tv_exp_f32 v109, v109\n\tv_exp_f32 v110, v110\n\t" \
    "v_mfma_f32_32x32x4_2b_bf16 v[64:95], v[202:203], v[200:201], v[64:95]\n\tds_read_b64 v[202:203], %[k2a] offset:7936\n\tv_exp_f32 v111, v111\n\t" \
    "v_exp_f32 v112, v112\n\tv_exp_f32 v113, v113\n\tv_exp_f32 v114, v114\n\tv_exp_f32 v115, v115\n\tv_exp_f32 v116, v116\n\tv_exp_f32 v117, v117\n\t" \
    "v_exp_f32 v118, v118\n\tv_smfmac_f32_32x32x32_bf16 v[128:143], v[220:223], v[104:111], v224\n\tv_exp_f32 v119, v119\n\tv_exp_f32 v120, v120\n\t" \
    "v_exp_f32 v121, v121\n\tv_exp_f32 v122, v122\n\tv_smfmac_f32_32x32x32_bf16 v[144:159], v[216:219], v[112:119], v224\n\tv_exp_f32 v123, v123\n\t" \
    "v_exp_f32 v124, v124\n\tv_exp_f32 v125, v125\n\tv_exp_f32 v126, v126\n\tv_exp_f32 v127, v127\n\t"
#define ASM_1G \
    "s_setprio 2\n\ts_waitcnt lgkmcnt(0)\n\tv_mfma_f32_32x32x16_bf16 v[96:111], v[204:207], v[192:195], v[160:175]\n\tv_exp_f32 v64, v64\n\t" \
    "v_exp_f32 v65, v65\n\tv_exp_f32 v66, v66\n\tv_smfmac_f32_32x32x32_bf16 v[144:159], v[220:223], v[120:127], v224\n\t" \
    "ds_read_b128 v[216:219], %[vp] offset:9408\n\tds_read_b128 v[220:223], %[vp] offset:10080\n\tv_exp_f32 v67, v67\n\tv_exp_f32 v68, v68\n\t" \
    "v_exp_f32 v69, v69\n\tv_exp_f32 v70, v70\n\tv_mfma_f32_32x32x16_bf16 v[112:127], v[204:207], v[196:199], v[176:191]\n\t" \
    "ds_read_b128 v[204:207], %[ka] offset:0\n\tv_exp_f32 v71, v71\n\tv_exp_f32 v72, v72\n\tv_exp_f32 v73, v73\n\tv_exp_f32 v74, v74\n\t" \
    "v_smfmac_f32_32x32x32_bf16 v[128:143], v[208:211], v[64:71], v224\n\tv_exp_f32 v75, v75\n\tv_exp_f32 v76, v76\n\tv_exp_f32 v77, v77\n\t" \
    "v_exp_f32 v78, v78\n\tv_mfma_f32_32x32x4_2b_bf16 v[96:127], v[202:203], v[200:201], v[96:127]\n\tds_read_b64 v[202:203], %[k2a] offset:0\n\t" \
    "v_exp_f32 v79, v79\n\tv_exp_f32 v80, v80\n\tv_exp_f32 v81, v81\n\tv_exp_f32 v82, v82\n\tv_exp_f32 v83, v83\n\tv_exp_f32 v84, v84\n\t" \
    "v_exp_f32 v85, v85\n\tv_exp_f32 v86, v86\n\tv_smfmac_f32_32x32x32_bf16 v[128:143], v[212:215], v[72:79], v224\n\tv_exp_f32 v87, v87\n\t" \
    "v_exp_f32 v88, v88\n\tv_exp_f32 v89, v89\n\tv_exp_f32 v90, v90\n\tv_smfmac_f32_32x32x32_bf16 v[144:159], v[208:211], v[80:87], v224\n\t" \
    "v_exp_f32 v91, v91\n\tv_exp_f32 v92, v92\n\tv_exp_f32 v93, v93\n\tv_exp_f32 v94, v94\n\tv_exp_f32 v95, v95\n\ts_waitcnt lgkmcnt(0)\n\t" \
    "v_mfma_f32_32x32x16_bf16 v[64:79], v[204:207], v[192:195], v[160:175]\n\tv_exp_f32 v96, v96\n\tv_exp_f32 v97, v97\n\tv_exp_f32 v98, v98\n\t" \
    "v_smfmac_f32_32x32x32_bf16 v[144:159], v[212:215], v[88:95], v224\n\tds_read_b128 v[208:211], %[va] offset:0\n\t" \
    "ds_read_b128 v[212:215], %[va] offset:672\n\tv_exp_f32 v99, v99\n\tv_exp_f32 v100, v100\n\tv_exp_f32 v101, v101\n\tv_exp_f32 v102, v102\n\t" \
    "v_mfma_f32_32x32x16_bf16 v[80:95], v[204:207], v[196:199], v[176:191]\n\tds_read_b128 v[204:207], %[ka] offset:1024\n\tv_exp_f32 v103, v103\n\t" \
    "v_exp_f32 v104, v104\n\tv_exp_f32 v105, v105\n\tv_exp_f32 v106, v106\n\tv_smfmac_f32_32x32x32_bf16 v[128:143], v[216:219], v[96:103], v224\n\t" \
    "v_exp_f32 v107, v107\n\tv_exp_f32 v108, v108\n\tv_exp_f32 v109, v109\n\tv_exp_f32 v110, v110\n\t" \
    "v_mfma_f32_32x32x4_2b_bf16 v[64:95], v[202:203], v[200:201], v[64:95]\n\tds_read_b64 v[202:203], %[k2a] offset:256\n\tv_exp_f32 v111, v111\n\t" \
    "v_exp_f32 v112, v112\n\tv_exp_f32 v113, v113\n\tv_exp_f32 v114, v114\n\tv_exp_f32 v115, v115\n\tv_exp_f32 v116, v116\n\tv_exp_f32 v117, v117\n\t" \
    "v_exp_f32 v118, v118\n\tv_smfmac_f32_32x32x32_bf16 v[128:143], v[220:223], v[104:111], v224\n\tv_exp_f32 v119, v119\n\tv_exp_f32 v120, v120\n\t" \
    "v_exp_f32 v121, v121\n\tv_exp_f32 v122, v122\n\tv_smfmac_f32_32x32x32_bf16 v[144:159], v[216:219], v[112:119], v224\n\tv_exp_f32 v123, v123\n\t" \
    "v_exp_f32 v124, v124\n\tv_exp_f32 v125, v125\n\tv_exp_f32 v126, v126\n\tv_exp_f32 v127, v127\n\t"
#define ASM_1C \
    "s_setprio 1\n\ts_waitcnt lgkmcnt(0)\n\tv_mfma_f32_32x32x16_bf16 v[96:111], v[204:207], v[192:195], v[160:175]\n\tv_exp_f32 v64, v64\n\t" \
    "v_exp_f32 v65, v65\n\tv_exp_f32 v66, v66\n\tv_smfmac_f32_32x32x32_bf16 v[144:159], v[220:223], v[120:127], v224\n\t" \
    "ds_read_b128 v[216:219], %[va] offset:4032\n\tds_read_b128 v[220:223], %[va] offset:4704\n\tv_exp_f32 v67, v67\n\tv_exp_f32 v68, v68\n\t" \
    "v_exp_f32 v69, v69\n\tv_exp_f32 v70, v70\n\tv_mfma_f32_32x32x16_bf16 v[112:127], v[204:207], v[196:199], v[176:191]\n\t" \
    "ds_read_b128 v[204:207], %[ka] offset:5120\n\tv_exp_f32 v71, v71\n\tv_exp_f32 v72, v72\n\tv_exp_f32 v73, v73\n\tv_exp_f32 v74, v74\n\t" \
    "v_smfmac_f32_32x32x32_bf16 v[128:143], v[208:211], v[64:71], v224\n\tglobal_load_lds_dwordx4 %[dv0], %[dg] offset:0 sc1\n\tv_exp_f32 v75, v75\n\t" \
    "v_exp_f32 v76, v76\n\tv_exp_f32 v77, v77\n\tv_exp_f32 v78, v78\n\tv_mfma_f32_32x32x4_2b_bf16 v[96:127], v[202:203], v[200:201], v[96:127]\n\t" \
    "ds_read_b64 v[202:203], %[k2a] offset:5120\n\tv_exp_f32 v79, v79\n\tv_exp_f32 v80, v80\n\tv_exp_f32 v81, v81\n\tv_exp_f32 v82, v82\n\t" \
    "v_exp_f32 v83, v83\n\tv_exp_f32 v84, v84\n\tv_exp_f32 v85, v85\n\tv_exp_f32 v86, v86\n\t" \
    "v_smfmac_f32_32x32x32_bf16 v[128:143], v[212:215], v[72:79], v224\n\tv_exp_f32 v87, v87\n\tv_exp_f32 v88, v88\n\tv_exp_f32 v89, v89\n\t" \
    "v_exp_f32 v90, v90\n\tv_smfmac_f32_32x32x32_bf16 v[144:159], v[208:211], v[80:87], v224\n\tv_exp_f32 v91, v91\n\tv_exp_f32 v92, v92\n\t" \
    "v_exp_f32 v93, v93\n\tv_exp_f32 v94, v94\n\tv_exp_f32 v95, v95\n\ts_waitcnt lgkmcnt(0)\n\t" \
    "v_mfma_f32_32x32x16_bf16 v[64:79], v[204:207], v[192:195], v[160:175]\n\tv_exp_f32 v96, v96\n\tv_exp_f32 v97, v97\n\tv_exp_f32 v98, v98\n\t" \
    "v_smfmac_f32_32x32x32_bf16 v[144:159], v[212:215], v[88:95], v224\n\tds_read_b128 v[208:211], %[va] offset:5376\n\t" \
    "ds_read_b128 v[212:215], %[va] offset:6048\n\tv_exp_f32 v99, v99\n\tv_exp_f32 v100, v100\n\tv_exp_f32 v101, v101\n\tv_exp_f32 v102, v102\n\t" \
    "v_mfma_f32_32x32x16_bf16 v[80:95], v[204:207], v[196:199], v[176:191]\n\tds_read_b128 v[204:207], %[ka] offset:6144\n\tv_exp_f32 v103, v103\n\t" \
    "v_exp_f32 v104, v104\n\tv_exp_f32 v105, v105\n\tv_exp_f32 v106, v106\n\tv_smfmac_f32_32x32x32_bf16 v[128:143], v[216:219], v[96:103], v224\n\t" \
    "global_load_lds_dwordx4 %[dv1], %[dg] offset:1024 sc1\n\tv_exp_f32 v107, v107\n\tv_exp_f32 v108, v108\n\tv_exp_f32 v109, v109\n\t" \
    "v_exp_f32 v110, v110\n\tv_mfma_f32_32x32x4_2b_bf16 v[64:95], v[202:203], v[200:201], v[64:95]\n\tds_read_b64 v[202:203], %[k2a] offset:5376\n\t" \
    "v_exp_f32 v111, v111\n\tv_exp_f32 v112, v112\n\tv_exp_f32 v113, v113\n\tv_exp_f32 v114, v114\n\tv_exp_f32 v115, v115\n\tv_exp_f32 v116, v116\n\t" \
    "v_exp_f32 v117, v117\n\tv_exp_f32 v118, v118\n\tv_smfmac_f32_32x32x32_bf16 v[128:143], v[220:223], v[104:111], v224\n\tv_exp_f32 v119, v119\n\t" \
    "v_exp_f32 v120, v120\n\tv_exp_f32 v121, v121\n\tv_exp_f32 v122, v122\n\tv_smfmac_f32_32x32x32_bf16 v[144:159], v[216:219], v[112:119], v224\n\t" \
    "v_exp_f32 v123, v123\n\tv_exp_f32 v124, v124\n\tv_exp_f32 v125, v125\n\tv_exp_f32 v126, v126\n\tv_exp_f32 v127, v127\n\ts_setprio 0\n\t" \
    "s_waitcnt lgkmcnt(0)\n\tv_mfma_f32_32x32x16_bf16 v[96:111], v[204:207], v[192:195], v[160:175]\n\tv_exp_f32 v64, v64\n\tv_exp_f32 v65, v65\n\t" \
    "v_exp_f32 v66, v66\n\tv_smfmac_f32_32x32x32_bf16 v[144:159], v[220:223], v[120:127], v224\n\tds_read_b128 v[216:219], %[va] offset:6720\n\t" \
    "ds_read_b128 v[220:223], %[va] offset:7392\n\tv_exp_f32 v67, v67\n\tv_exp_f32 v68, v68\n\tv_exp_f32 v69, v69\n\tv_exp_f32 v70, v70\n\t" \
    "v_mfma_f32_32x32x16_bf16 v[112:127], v[204:207], v[196:199], v[176:191]\n\tds_read_b128 v[204:207], %[ka] offset:7680\n\tv_exp_f32 v71, v71\n\t" \
    "v_exp_f32 v72, v72\n\tv_exp_f32 v73, v73\n\tv_exp_f32 v74, v74\n\tv_smfmac_f32_32x32x32_bf16 v[128:143], v[208:211], v[64:71], v224\n\t" \
    "global_load_lds_dwordx4 %[dv2], %[dg] offset:2048 sc1\n\tv_exp_f32 v75, v75\n\tv_exp_f32 v76, v76\n\tv_exp_f32 v77, v77\n\tv_exp_f32 v78, v78\n\t" \
    "v_mfma_f32_32x32x4_2b_bf16 v[96:127], v[202:203], v[200:201], v[96:127]\n\tds_read_b64 v[202:203], %[k2a] offset:7680\n\tv_exp_f32 v79, v79\n\t" \
    "v_exp_f32 v80, v80\n\tv_exp_f32 v81, v81\n\tv_exp_f32 v82, v82\n\tv_exp_f32 v83, v83\n\tv_exp_f32 v84, v84\n\tv_exp_f32 v85, v85\n\t" \
    "v_exp_f32 v86, v86\n\tv_smfmac_f32_32x32x32_bf16 v[128:143], v[212:215], v[72:79], v224\n\tv_exp_f32 v87, v87\n\tv_exp_f32 v88, v88\n\t" \
    "v_exp_f32 v89, v89\n\tv_exp_f32 v90, v90\n\tv_smfmac_f32_32x32x32_bf16 v[144:159], v[208:211], v[80:87], v224\n\tv_exp_f32 v91, v91\n\t" \
    "v_exp_f32 v92, v92\n\tv_exp_f32 v93, v93\n\tv_exp_f32 v94, v94\n\tv_exp_f32 v95, v95\n\ts_waitcnt lgkmcnt(0)\n\t" \
    "v_mfma_f32_32x32x16_bf16 v[64:79], v[204:207], v[192:195], v[160:175]\n\tv_exp_f32 v96, v96\n\tv_exp_f32 v97, v97\n\tv_exp_f32 v98, v98\n\t" \
    "v_smfmac_f32_32x32x32_bf16 v[144:159], v[212:215], v[88:95], v224\n\tds_read_b128 v[208:211], %[va] offset:8064\n\t" \
    "ds_read_b128 v[212:215], %[va] offset:8736\n\tv_exp_f32 v99, v99\n\tv_exp_f32 v100, v100\n\tv_exp_f32 v101, v101\n\tv_exp_f32 v102, v102\n\t" \
    "v_mfma_f32_32x32x16_bf16 v[80:95], v[204:207], v[196:199], v[176:191]\n\tds_read_b128 v[204:207], %[ka] offset:8704\n\tv_exp_f32 v103, v103\n\t" \
    "v_exp_f32 v104, v104\n\tv_exp_f32 v105, v105\n\tv_exp_f32 v106, v106\n\tv_smfmac_f32_32x32x32_bf16 v[128:143], v[216:219], v[96:103], v224\n\t" \
    "v_exp_f32 v107, v107\n\tv_exp_f32 v108, v108\n\tv_exp_f32 v109, v109\n\tv_exp_f32 v110, v110\n\t" \
    "v_mfma_f32_32x32x4_2b_bf16 v[64:95], v[202:203], v[200:201], v[64:95]\n\tds_read_b64 v[202:203], %[k2a] offset:7936\n\tv_exp_f32 v111, v111\n\t" \
    "v_exp_f32 v112, v112\n\tv_exp_f32 v113, v113\n\tv_exp_f32 v114, v114\n\tv_exp_f32 v115, v115\n\tv_exp_f32 v116, v116\n\tv_exp_f32 v117, v117\n\t" \
    "v_exp_f32 v118, v118\n\tv_smfmac_f32_32x32x32_bf16 v[128:143], v[220:223], v[104:111], v224\n\tv_exp_f32 v119, v119\n\tv_exp_f32 v120, v120\n\t" \
    "v_exp_f32 v121, v121\n\tv_exp_f32 v122, v122\n\tv_smfmac_f32_32x32x32_bf16 v[144:159], v[216:219], v[112:119], v224\n\tv_exp_f32 v123, v123\n\t" \
    "v_exp_f32 v124, v124\n\tv_exp_f32 v125, v125\n\tv_exp_f32 v126, v126\n\tv_exp_f32 v127, v127\n\t"
#define ASM_1D \
    "s_setprio 1\n\ts_waitcnt lgkmcnt(0)\n\tv_mfma_f32_32x32x16_bf16 v[96:111], v[204:207], v[192:195], v[160:175]\n\tv_exp_f32 v64, v64\n\t" \
    "v_exp_f32 v65, v65\n\tv_exp_f32 v66, v66\n\tv_smfmac_f32_32x32x32_bf16 v[144:159], v[220:223], v[120:127], v224\n\t" \
    "ds_read_b128 v[216:219], %[va] offset:4032\n\tds_read_b128 v[220:223], %[va] offset:4704\n\tv_exp_f32 v67, v67\n\tv_exp_f32 v68, v68\n\t" \
    "v_exp_f32 v69, v69\n\tv_exp_f32 v70, v70\n\tv_mfma_f32_32x32x16_bf16 v[112:127], v[204:207], v[196:199], v[176:191]\n\t" \
    "ds_read_b128 v[204:207], %[ka] offset:5120\n\tv_exp_f32 v71, v71\n\tv_exp_f32 v72, v72\n\tv_exp_f32 v73, v73\n\tv_exp_f32 v74, v74\n\t" \
    "v_smfmac_f32_32x32x32_bf16 v[128:143], v[208:211], v[64:71], v224\n\tglobal_load_lds_dwordx4 %[dv0], %[dg] offset:0 sc1\n\tv_exp_f32 v75, v75\n\t" \
    "v_exp_f32 v76, v76\n\tv_exp_f32 v77, v77\n\tv_exp_f32 v78, v78\n\tv_mfma_f32_32x32x4_2b_bf16 v[96:127], v[202:203], v[200:201], v[96:127]\n\t" \
    "ds_read_b64 v[202:203], %[k2a] offset:5120\n\tv_exp_f32 v79, v79\n\tv_exp_f32 v80, v80\n\tv_exp_f32 v81, v81\n\tv_exp_f32 v82, v82\n\t" \
    "v_exp_f32 v83, v83\n\tv_exp_f32 v84, v84\n\tv_exp_f32 v85, v85\n\tv_exp_f32 v86, v86\n\t" \
    "v_smfmac_f32_32x32x32_bf16 v[128:143], v[212:215], v[72:79], v224\n\tv_exp_f32 v87, v87\n\tv_exp_f32 v88, v88\n\tv_exp_f32 v89, v89\n\t" \
    "v_exp_f32 v90, v90\n\tv_smfmac_f32_32x32x32_bf16 v[144:159], v[208:211], v[80:87], v224\n\tv_exp_f32 v91, v91\n\tv_exp_f32 v92, v92\n\t" \
    "v_exp_f32 v93, v93\n\tv_exp_f32 v94, v94\n\tv_exp_f32 v95, v95\n\ts_waitcnt lgkmcnt(0)\n\t" \
    "v_mfma_f32_32x32x16_bf16 v[64:79], v[204:207], v[192:195], v[160:175]\n\tv_exp_f32 v96, v96\n\tv_exp_f32 v97, v97\n\tv_exp_f32 v98, v98\n\t" \
    "v_smfmac_f32_32x32x32_bf16 v[144:159], v[212:215], v[88:95], v224\n\tds_read_b128 v[208:211], %[va] offset:5376\n\t" \
    "ds_read_b128 v[212:215], %[va] offset:6048\n\tv_exp_f32 v99, v99\n\tv_exp_f32 v100, v100\n\tv_exp_f32 v101, v101\n\tv_exp_f32 v102, v102\n\t" \
    "v_mfma_f32_32x32x16_bf16 v[80:95], v[204:207], v[196:199], v[176:191]\n\tds_read_b128 v[204:207], %[ka] offset:6144\n\tv_exp_f32 v103, v103\n\t" \
    "v_exp_f32 v104, v104\n\tv_exp_f32 v105, v105\n\tv_exp_f32 v106, v106\n\tv_smfmac_f32_32x32x32_bf16 v[128:143], v[216:219], v[96:103], v224\n\t" \
    "v_exp_f32 v107, v107\n\tv_exp_f32 v108, v108\n\tv_exp_f32 v109, v109\n\tv_exp_f32 v110, v110\n\t" \
    "v_mfma_f32_32x32x4_2b_bf16 v[64:95], v[202:203], v[200:201], v[64:95]\n\tds_read_b64 v[202:203], %[k2a] offset:5376\n\tv_exp_f32 v111, v111\n\t" \
    "v_exp_f32 v112, v112\n\tv_exp_f32 v113, v113\n\tv_exp_f32 v114, v114\n\tv_exp_f32 v115, v115\n\tv_exp_f32 v116, v116\n\tv_exp_f32 v117, v117\n\t" \
    "v_exp_f32 v118, v118\n\tv_smfmac_f32_32x32x32_bf16 v[128:143], v[220:223], v[104:111], v224\n\tv_exp_f32 v119, v119\n\tv_exp_f32 v120, v120\n\t" \
    "v_exp_f32 v121, v121\n\tv_exp_f32 v122, v122\n\tv_smfmac_f32_32x32x32_bf16 v[144:159], v[216:219], v[112:119], v224\n\tv_exp_f32 v123, v123\n\t" \
    "v_exp_f32 v124, v124\n\tv_exp_f32 v125, v125\n\tv_exp_f32 v126, v126\n\tv_exp_f32 v127, v127\n\t"
#define ASM_1E \
    "s_setprio 0\n\ts_waitcnt lgkmcnt(0)\n\tv_mfma_f32_32x32x16_bf16 v[96:111], v[204:207], v[192:195], v[160:175]\n\tv_exp_f32 v64, v64\n\t" \
    "v_exp_f32 v65, v65\n\tv_exp_f32 v66, v66\n\tv_smfmac_f32_32x32x32_bf16 v[144:159], v[220:223], v[120:127], v224\n\t" \
    "ds_read_b128 v[216:219], %[va] offset:6720\n\tds_read_b128 v[220:223], %[va] offset:7392\n\tv_exp_f32 v67, v67\n\tv_exp_f32 v68, v68\n\t" \
    "v_exp_f32 v69, v69\n\tv_exp_f32 v70, v70\n\tv_mfma_f32_32x32x16_bf16 v[112:127], v[204:207], v[196:199], v[176:191]\n\t" \
    "ds_read_b128 v[204:207], %[ka] offset:7680\n\tv_exp_f32 v71, v71\n\tv_exp_f32 v72, v72\n\tv_exp_f32 v73, v73\n\tv_exp_f32 v74, v74\n\t" \
    "v_smfmac_f32_32x32x32_bf16 v[128:143], v[208:211], v[64:71], v224\n\tv_exp_f32 v75, v75\n\tv_exp_f32 v76, v76\n\tv_exp_f32 v77, v77\n\t" \
    "v_exp_f32 v78, v78\n\tv_mfma_f32_32x32x4_2b_bf16 v[96:127], v[202:203], v[200:201], v[96:127]\n\tds_read_b64 v[202:203], %[k2a] offset:7680\n\t" \
    "v_exp_f32 v79, v79\n\tv_exp_f32 v80, v80\n\tv_exp_f32 v81, v81\n\tv_exp_f32 v82, v82\n\tv_exp_f32 v83, v83\n\tv_exp_f32 v84, v84\n\t" \
    "v_exp_f32 v85, v85\n\tv_exp_f32 v86, v86\n\tv_smfmac_f32_32x32x32_bf16 v[128:143], v[212:215], v[72:79], v224\n\tv_exp_f32 v87, v87\n\t" \
    "v_exp_f32 v88, v88\n\tv_exp_f32 v89, v89\n\tv_exp_f32 v90, v90\n\tv_smfmac_f32_32x32x32_bf16 v[144:159], v[208:211], v[80:87], v224\n\t" \
    "v_exp_f32 v91, v91\n\tv_exp_f32 v92, v92\n\tv_exp_f32 v93, v93\n\tv_exp_f32 v94, v94\n\tv_exp_f32 v95, v95\n\ts_waitcnt lgkmcnt(0)\n\t" \
    "v_mfma_f32_32x32x16_bf16 v[64:79], v[204:207], v[192:195], v[160:175]\n\tv_exp_f32 v96, v96\n\tv_exp_f32 v97, v97\n\tv_exp_f32 v98, v98\n\t" \
    "v_smfmac_f32_32x32x32_bf16 v[144:159], v[212:215], v[88:95], v224\n\tds_read_b128 v[208:211], %[va] offset:8064\n\t" \
    "ds_read_b128 v[212:215], %[va] offset:8736\n\tv_exp_f32 v99, v99\n\tv_exp_f32 v100, v100\n\tv_exp_f32 v101, v101\n\tv_exp_f32 v102, v102\n\t" \
    "v_mfma_f32_32x32x16_bf16 v[80:95], v[204:207], v[196:199], v[176:191]\n\tds_read_b128 v[204:207], %[ka] offset:8704\n\tv_exp_f32 v103, v103\n\t" \
    "v_exp_f32 v104, v104\n\tv_exp_f32 v105, v105\n\tv_exp_f32 v106, v106\n\tv_smfmac_f32_32x32x32_bf16 v[128:143], v[216:219], v[96:103], v224\n\t" \
    "v_exp_f32 v107, v107\n\tv_exp_f32 v108, v108\n\tv_exp_f32 v109, v109\n\tv_exp_f32 v110, v110\n\t" \
    "v_mfma_f32_32x32x4_2b_bf16 v[64:95], v[202:203], v[200:201], v[64:95]\n\tds_read_b64 v[202:203], %[k2a] offset:7936\n\tv_exp_f32 v111, v111\n\t" \
    "v_exp_f32 v112, v112\n\tv_exp_f32 v113, v113\n\tv_exp_f32 v114, v114\n\tv_exp_f32 v115, v115\n\tv_exp_f32 v116, v116\n\tv_exp_f32 v117, v117\n\t" \
    "v_exp_f32 v118, v118\n\tv_smfmac_f32_32x32x32_bf16 v[128:143], v[220:223], v[104:111], v224\n\tv_exp_f32 v119, v119\n\tv_exp_f32 v120, v120\n\t" \
    "v_exp_f32 v121, v121\n\tv_exp_f32 v122, v122\n\tv_smfmac_f32_32x32x32_bf16 v[144:159], v[216:219], v[112:119], v224\n\tv_exp_f32 v123, v123\n\t" \
    "v_exp_f32 v124, v124\n\tv_exp_f32 v125, v125\n\tv_exp_f32 v126, v126\n\tv_exp_f32 v127, v127\n\t"
#define ASM_3H \
    "s_setprio 1\n\ts_waitcnt lgkmcnt(0)\n\tv_mfma_f32_32x32x16_bf16 v[96:111], v[204:207], v[192:195], v[160:175]\n\tv_exp_f32 v64, v64\n\t" \
    "v_exp_f32 v65, v65\n\tv_exp_f32 v66, v66\n\tv_smfmac_f32_32x32x32_bf16 v[144:159], v[220:223], v[120:127], v224\n\t" \
    "ds_read_b128 v[216:219], %[vp] offset:9408\n\tds_read_b128 v[220:223], %[vp] offset:10080\n\tv_exp_f32 v67, v67\n\tv_exp_f32 v68, v68\n\t" \
    "v_exp_f32 v69, v69\n\tv_exp_f32 v70, v70\n\tv_mfma_f32_32x32x16_bf16 v[112:127], v[204:207], v[196:199], v[176:191]\n\t" \
    "ds_read_b128 v[204:207], %[ka] offset:0\n\tv_exp_f32 v71, v71\n\tv_exp_f32 v72, v72\n\tv_exp_f32 v73, v73\n\tv_exp_f32 v74, v74\n\t" \
    "v_smfmac_f32_32x32x32_bf16 v[128:143], v[208:211], v[64:71], v224\n\tv_exp_f32 v75, v75\n\tv_exp_f32 v76, v76\n\tv_exp_f32 v77, v77\n\t" \
    "v_exp_f32 v78, v78\n\tv_mfma_f32_32x32x4_2b_bf16 v[96:127], v[202:203], v[200:201], v[96:127]\n\tds_read_b64 v[202:203], %[k2a] offset:0\n\t" \
    "v_exp_f32 v79, v79\n\tv_exp_f32 v80, v80\n\tv_exp_f32 v81, v81\n\tv_exp_f32 v82, v82\n\tv_exp_f32 v83, v83\n\tv_exp_f32 v84, v84\n\t" \
    "v_exp_f32 v85, v85\n\tv_exp_f32 v86, v86\n\tv_smfmac_f32_32x32x32_bf16 v[128:143], v[212:215], v[72:79], v224\n\tv_exp_f32 v87, v87\n\t" \
    "v_exp_f32 v88, v88\n\tv_exp_f32 v89, v89\n\tv_exp_f32 v90, v90\n\tv_smfmac_f32_32x32x32_bf16 v[144:159], v[208:211], v[80:87], v224\n\t" \
    "v_exp_f32 v91, v91\n\tv_exp_f32 v92, v92\n\tv_exp_f32 v93, v93\n\tv_exp_f32 v94, v94\n\tv_exp_f32 v95, v95\n\ts_waitcnt lgkmcnt(0)\n\t" \
    "v_mfma_f32_32x32x16_bf16 v[64:79], v[204:207], v[192:195], v[160:175]\n\tv_exp_f32 v96, v96\n\tv_exp_f32 v97, v97\n\tv_exp_f32 v98, v98\n\t" \
    "v_smfmac_f32_32x32x32_bf16 v[144:159], v[212:215], v[88:95], v224\n\tds_read_b128 v[208:211], %[va] offset:0\n\t" \
    "ds_read_b128 v[212:215], %[va] offset:672\n\tv_exp_f32 v99, v99\n\tv_exp_f32 v100, v100\n\tv_exp_f32 v101, v101\n\tv_exp_f32 v102, v102\n\t" \
    "v_mfma_f32_32x32x16_bf16 v[80:95], v[204:207], v[196:199], v[176:191]\n\tds_read_b128 v[204:207], %[ka] offset:1024\n\tv_exp_f32 v103, v103\n\t" \
    "v_exp_f32 v104, v104\n\tv_exp_f32 v105, v105\n\tv_exp_f32 v106, v106\n\tv_smfmac_f32_32x32x32_bf16 v[128:143], v[216:219], v[96:103], v224\n\t" \
    "v_exp_f32 v107, v107\n\tv_exp_f32 v108, v108\n\tv_exp_f32 v109, v109\n\tv_exp_f32 v110, v110\n\t" \
    "v_mfma_f32_32x32x4_2b_bf16 v[64:95], v[202:203], v[200:201], v[64:95]\n\tds_read_b64 v[202:203], %[k2a] offset:256\n\tv_exp_f32 v111, v111\n\t" \
    "v_exp_f32 v112, v112\n\tv_exp_f32 v113, v113\n\tv_exp_f32 v114, v114\n\tv_exp_f32 v115, v115\n\tv_exp_f32 v116, v116\n\tv_exp_f32 v117, v117\n\t" \
    "v_exp_f32 v118, v118\n\tv_smfmac_f32_32x32x32_bf16 v[128:143], v[220:223], v[104:111], v224\n\tv_exp_f32 v119, v119\n\tv_exp_f32 v120, v120\n\t" \
    "v_exp_f32 v121, v121\n\tv_exp_f32 v122, v122\n\tv_smfmac_f32_32x32x32_bf16 v[144:159], v[216:219], v[112:119], v224\n\tv_exp_f32 v123, v123\n\t" \
    "v_exp_f32 v124, v124\n\tv_exp_f32 v125, v125\n\tv_exp_f32 v126, v126\n\tv_exp_f32 v127, v127\n\t"
#define ASM_3G \
    "s_setprio 1\n\ts_waitcnt lgkmcnt(0)\n\tv_mfma_f32_32x32x16_bf16 v[96:111], v[204:207], v[192:195], v[160:175]\n\tv_exp_f32 v64, v64\n\t" \
    "v_exp_f32 v65, v65\n\tv_exp_f32 v66, v66\n\tv_smfmac_f32_32x32x32_bf16 v[144:159], v[220:223], v[120:127], v224\n\t" \
    "ds_read_b128 v[216:219], %[vp] offset:9408\n\tds_read_b128 v[220:223], %[vp] offset:10080\n\tv_exp_f32 v67, v67\n\tv_exp_f32 v68, v68\n\t" \
    "v_exp_f32 v69, v69\n\tv_exp_f32 v70, v70\n\tv_mfma_f32_32x32x16_bf16 v[112:127], v[204:207], v[196:199], v[176:191]\n\t" \
    "ds_read_b128 v[204:207], %[ka] offset:0\n\tv_exp_f32 v71, v71\n\tv_exp_f32 v72, v72\n\tv_exp_f32 v73, v73\n\tv_exp_f32 v74, v74\n\t" \
    "v_smfmac_f32_32x32x32_bf16 v[128:143], v[208:211], v[64:71], v224\n\tbuffer_load_dwordx4 %[dv], %[rs], %[so] offen offset:0 sc1 lds\n\t" \
    "v_exp_f32 v75, v75\n\tv_exp_f32 v76, v76\n\tv_exp_f32 v77, v77\n\tv_exp_f32 v78, v78\n\t" \
    "v_mfma_f32_32x32x4_2b_bf16 v[96:127], v[202:203], v[200:201], v[96:127]\n\tds_read_b64 v[202:203], %[k2a] offset:0\n\tv_exp_f32 v79, v79\n\t" \
    "v_exp_f32 v80, v80\n\tv_exp_f32 v81, v81\n\tv_exp_f32 v82, v82\n\tv_exp_f32 v83, v83\n\tv_exp_f32 v84, v84\n\tv_exp_f32 v85, v85\n\t" \
    "v_exp_f32 v86, v86\n\tv_smfmac_f32_32x32x32_bf16 v[128:143], v[212:215], v[72:79], v224\n\t" \
    "buffer_load_dwordx4 %[dv], %[rs], %[so] offen offset:1024 sc1 lds\n\tv_exp_f32 v87, v87\n\tv_exp_f32 v88, v88\n\tv_exp_f32 v89, v89\n\t" \
    "v_exp_f32 v90, v90\n\tv_smfmac_f32_32x32x32_bf16 v[144:159], v[208:211], v[80:87], v224\n\t" \
    "buffer_load_dwordx4 %[dv], %[rs], %[so] offen offset:2048 sc1 lds\n\tv_exp_f32 v91, v91\n\tv_exp_f32 v92, v92\n\tv_exp_f32 v93, v93\n\t" \
    "v_exp_f32 v94, v94\n\tv_exp_f32 v95, v95\n\ts_waitcnt lgkmcnt(0)\n\tv_mfma_f32_32x32x16_bf16 v[64:79], v[204:207], v[192:195], v[160:175]\n\t" \
    "v_exp_f32 v96, v96\n\tv_exp_f32 v97, v97\n\tv_exp_f32 v98, v98\n\tv_smfmac_f32_32x32x32_bf16 v[144:159], v[212:215], v[88:95], v224\n\t" \
    "ds_read_b128 v[208:211], %[va] offset:0\n\tds_read_b128 v[212:215], %[va] offset:672\n\tv_exp_f32 v99, v99\n\tv_exp_f32 v100, v100\n\t" \
    "v_exp_f32 v101, v101\n\tv_exp_f32 v102, v102\n\tv_mfma_f32_32x32x16_bf16 v[80:95], v[204:207], v[196:199], v[176:191]\n\t" \
    "ds_read_b128 v[204:207], %[ka] offset:1024\n\tv_exp_f32 v103, v103\n\tv_exp_f32 v104, v104\n\tv_exp_f32 v105, v105\n\tv_exp_f32 v106, v106\n\t" \
    "v_smfmac_f32_32x32x32_bf16 v[128:143], v[216:219], v[96:103], v224\n\tbuffer_load_dwordx4 %[dv], %[rs], %[so] offen offset:3072 sc1 lds\n\t" \
    "s_add_u32 %[so], %[so], 0x1000\n\ts_add_u32 m0, m0, 0x1000\n\tv_exp_f32 v107, v107\n\tv_exp_f32 v108, v108\n\tv_exp_f32 v109, v109\n\t" \
    "v_exp_f32 v110, v110\n\tv_mfma_f32_32x32x4_2b_bf16 v[64:95], v[202:203], v[200:201], v[64:95]\n\tds_read_b64 v[202:203], %[k2a] offset:256\n\t" \
    "v_exp_f32 v111, v111\n\tv_exp_f32 v112, v112\n\tv_exp_f32 v113, v113\n\tv_exp_f32 v114, v114\n\tv_exp_f32 v115, v115\n\tv_exp_f32 v116, v116\n\t" \
    "v_exp_f32 v117, v117\n\tv_exp_f32 v118, v118\n\tv_smfmac_f32_32x32x32_bf16 v[128:143], v[220:223], v[104:111], v224\n\tv_exp_f32 v119, v119\n\t" \
    "v_exp_f32 v120, v120\n\tv_exp_f32 v121, v121\n\tv_exp_f32 v122, v122\n\tv_smfmac_f32_32x32x32_bf16 v[144:159], v[216:219], v[112:119], v224\n\t" \
    "buffer_load_dwordx4 %[dv], %[rs], %[so] offen offset:0 sc1 lds\n\tv_exp_f32 v123, v123\n\tv_exp_f32 v124, v124\n\tv_exp_f32 v125, v125\n\t" \
    "v_exp_f32 v126, v126\n\tv_exp_f32 v127, v127\n\t"
#define ASM_3A \
    "s_setprio 2\n\ts_waitcnt lgkmcnt(0)\n\tv_mfma_f32_32x32x16_bf16 v[96:111], v[204:207], v[192:195], v[160:175]\n\tv_exp_f32 v64, v64\n\t" \
    "v_exp_f32 v65, v65\n\tv_exp_f32 v66, v66\n\tv_smfmac_f32_32x32x32_bf16 v[144:159], v[220:223], v[120:127], v224\n\t" \
    "ds_read_b128 v[216:219], %[va] offset:1344\n\tds_read_b128 v[220:223], %[va] offset:2016\n\tv_exp_f32 v67, v67\n\tv_exp_f32 v68, v68\n\t" \
    "v_exp_f32 v69, v69\n\tv_exp_f32 v70, v70\n\tv_mfma_f32_32x32x16_bf16 v[112:127], v[204:207], v[196:199], v[176:191]\n\t" \
    "ds_read_b128 v[204:207], %[ka] offset:2560\n\tv_exp_f32 v71, v71\n\tv_exp_f32 v72, v72\n\tv_exp_f32 v73, v73\n\tv_exp_f32 v74, v74\n\t" \
    "v_smfmac_f32_32x32x32_bf16 v[128:143], v[208:211], v[64:71], v224\n\tbuffer_load_dwordx4 %[dv], %[rs], %[so] offen offset:0 sc1 lds\n\t" \
    "v_exp_f32 v75, v75\n\tv_exp_f32 v76, v76\n\tv_exp_f32 v77, v77\n\tv_exp_f32 v78, v78\n\t" \
    "v_mfma_f32_32x32x4_2b_bf16 v[96:127], v[202:203], v[200:201], v[96:127]\n\tds_read_b64 v[202:203], %[k2a] offset:2560\n\tv_exp_f32 v79, v79\n\t" \
    "v_exp_f32 v80, v80\n\tv_exp_f32 v81, v81\n\tv_exp_f32 v82, v82\n\tv_exp_f32 v83, v83\n\tv_exp_f32 v84, v84\n\tv_exp_f32 v85, v85\n\t" \
    "v_exp_f32 v86, v86\n\tv_smfmac_f32_32x32x32_bf16 v[128:143], v[212:215], v[72:79], v224\n\t" \
    "buffer_load_dwordx4 %[dv], %[rs], %[so] offen offset:1024 sc1 lds\n\tv_exp_f32 v87, v87\n\tv_exp_f32 v88, v88\n\tv_exp_f32 v89, v89\n\t" \
    "v_exp_f32 v90, v90\n\tv_smfmac_f32_32x32x32_bf16 v[144:159], v[208:211], v[80:87], v224\n\t" \
    "buffer_load_dwordx4 %[dv], %[rs], %[so] offen offset:2048 sc1 lds\n\tv_exp_f32 v91, v91\n\tv_exp_f32 v92, v92\n\tv_exp_f32 v93, v93\n\t" \
    "v_exp_f32 v94, v94\n\tv_exp_f32 v95, v95\n\ts_waitcnt lgkmcnt(0)\n\tv_mfma_f32_32x32x16_bf16 v[64:79], v[204:207], v[192:195], v[160:175]\n\t" \
    "v_exp_f32 v96, v96\n\tv_exp_f32 v97, v97\n\tv_exp_f32 v98, v98\n\tv_smfmac_f32_32x32x32_bf16 v[144:159], v[212:215], v[88:95], v224\n\t" \
    "ds_read_b128 v[208:211], %[va] offset:2688\n\tds_read_b128 v[212:215], %[va] offset:3360\n\tv_exp_f32 v99, v99\n\tv_exp_f32 v100, v100\n\t" \
    "v_exp_f32 v101, v101\n\tv_exp_f32 v102, v102\n\tv_mfma_f32_32x32x16_bf16 v[80:95], v[204:207], v[196:199], v[176:191]\n\t" \
    "ds_read_b128 v[204:207], %[ka] offset:3584\n\tv_exp_f32 v103, v103\n\tv_exp_f32 v104, v104\n\tv_exp_f32 v105, v105\n\tv_exp_f32 v106, v106\n\t" \
    "v_smfmac_f32_32x32x32_bf16 v[128:143], v[216:219], v[96:103], v224\n\tbuffer_load_dwordx4 %[dv], %[rs], %[so] offen offset:3072 sc1 lds\n\t" \
    "s_add_u32 %[so], %[so], 0x1000\n\ts_add_u32 m0, m0, 0x1000\n\tv_exp_f32 v107, v107\n\tv_exp_f32 v108, v108\n\tv_exp_f32 v109, v109\n\t" \
    "v_exp_f32 v110, v110\n\tv_mfma_f32_32x32x4_2b_bf16 v[64:95], v[202:203], v[200:201], v[64:95]\n\tds_read_b64 v[202:203], %[k2a] offset:2816\n\t" \
    "v_exp_f32 v111, v111\n\tv_exp_f32 v112, v112\n\tv_exp_f32 v113, v113\n\tv_exp_f32 v114, v114\n\tv_exp_f32 v115, v115\n\tv_exp_f32 v116, v116\n\t" \
    "v_exp_f32 v117, v117\n\tv_exp_f32 v118, v118\n\tv_smfmac_f32_32x32x32_bf16 v[128:143], v[220:223], v[104:111], v224\n\tv_exp_f32 v119, v119\n\t" \
    "v_exp_f32 v120, v120\n\tv_exp_f32 v121, v121\n\tv_exp_f32 v122, v122\n\tv_smfmac_f32_32x32x32_bf16 v[144:159], v[216:219], v[112:119], v224\n\t" \
    "buffer_load_dwordx4 %[dv], %[rs], %[so] offen offset:0 sc1 lds\n\tv_exp_f32 v123, v123\n\tv_exp_f32 v124, v124\n\tv_exp_f32 v125, v125\n\t" \
    "v_exp_f32 v126, v126\n\tv_exp_f32 v127, v127\n\t"
#define ASM_3B \
    "s_setprio 2\n\ts_waitcnt lgkmcnt(0)\n\tv_mfma_f32_32x32x16_bf16 v[96:111], v[204:207], v[192:195], v[160:175]\n\tv_exp_f32 v64, v64\n\t" \
    "v_exp_f32 v65, v65\n\tv_exp_f32 v66, v66\n\tv_smfmac_f32_32x32x32_bf16 v[144:159], v[220:223], v[120:127], v224\n\t" \
    "ds_read_b128 v[216:219], %[va] offset:4032\n\tds_read_b128 v[220:223], %[va] offset:4704\n\tv_exp_f32 v67, v67\n\tv_exp_f32 v68, v68\n\t" \
    "v_exp_f32 v69, v69\n\tv_exp_f32 v70, v70\n\tv_mfma_f32_32x32x16_bf16 v[112:127], v[204:207], v[196:199], v[176:191]\n\t" \
    "ds_read_b128 v[204:207], %[ka] offset:5120\n\tbuffer_load_dwordx4 v[4:7], %[dv], %[rs], %[so] offen offset:0 sc1\n\tv_exp_f32 v71, v71\n\t" \
    "v_exp_f32 v72, v72\n\tv_exp_f32 v73, v73\n\tv_exp_f32 v74, v74\n\tv_smfmac_f32_32x32x32_bf16 v[128:143], v[208:211], v[64:71], v224\n\t" \
    "buffer_load_dwordx4 v[8:11], %[dv], %[rs], %[so] offen offset:1024 sc1\n\tv_exp_f32 v75, v75\n\tv_exp_f32 v76, v76\n\tv_exp_f32 v77, v77\n\t" \
    "v_exp_f32 v78, v78\n\tv_mfma_f32_32x32x4_2b_bf16 v[96:127], v[202:203], v[200:201], v[96:127]\n\tds_read_b64 v[202:203], %[k2a] offset:5120\n\t" \
    "v_exp_f32 v79, v79\n\tv_exp_f32 v80, v80\n\tv_exp_f32 v81, v81\n\tv_exp_f32 v82, v82\n\tv_exp_f32 v83, v83\n\tv_exp_f32 v84, v84\n\t" \
    "v_exp_f32 v85, v85\n\tv_exp_f32 v86, v86\n\tv_smfmac_f32_32x32x32_bf16 v[128:143], v[212:215], v[72:79], v224\n\t" \
    "buffer_load_dwordx4 v[12:15], %[dv], %[rs], %[so] offen offset:2048 sc1\n\tv_exp_f32 v87, v87\n\tv_exp_f32 v88, v88\n\tv_exp_f32 v89, v89\n\t" \
    "v_exp_f32 v90, v90\n\tv_smfmac_f32_32x32x32_bf16 v[144:159], v[208:211], v[80:87], v224\n\t" \
    "buffer_load_dwordx4 v[16:19], %[dv], %[rs], %[so] offen offset:3072 sc1\n\ts_add_u32 %[so], %[so], 0x1000\n\tv_exp_f32 v91, v91\n\t" \
    "v_exp_f32 v92, v92\n\tv_exp_f32 v93, v93\n\tv_exp_f32 v94, v94\n\tv_exp_f32 v95, v95\n\ts_setprio 1\n\ts_waitcnt lgkmcnt(0)\n\t" \
    "v_mfma_f32_32x32x16_bf16 v[64:79], v[204:207], v[192:195], v[160:175]\n\tv_exp_f32 v96, v96\n\tv_exp_f32 v97, v97\n\tv_exp_f32 v98, v98\n\t" \
    "v_smfmac_f32_32x32x32_bf16 v[144:159], v[212:215], v[88:95], v224\n\tds_read_b128 v[208:211], %[va] offset:5376\n\t" \
    "ds_read_b128 v[212:215], %[va] offset:6048\n\tv_exp_f32 v99, v99\n\tv_exp_f32 v100, v100\n\tv_exp_f32 v101, v101\n\tv_exp_f32 v102, v102\n\t" \
    "v_mfma_f32_32x32x16_bf16 v[80:95], v[204:207], v[196:199], v[176:191]\n\tds_read_b128 v[204:207], %[ka] offset:6144\n\t" \
    "buffer_load_dwordx4 v[20:23], %[dv], %[rs], %[so] offen offset:0 sc1\n\tv_exp_f32 v103, v103\n\tv_exp_f32 v104, v104\n\tv_exp_f32 v105, v105\n\t" \
    "v_exp_f32 v106, v106\n\tv_smfmac_f32_32x32x32_bf16 v[128:143], v[216:219], v[96:103], v224\n\t" \
    "buffer_load_dwordx4 v[24:27], %[dv], %[rs], %[so] offen offset:1024 sc1\n\tv_exp_f32 v107, v107\n\tv_exp_f32 v108, v108\n\tv_exp_f32 v109, v109\n\t" \
    "v_exp_f32 v110, v110\n\tv_mfma_f32_32x32x4_2b_bf16 v[64:95], v[202:203], v[200:201], v[64:95]\n\tds_read_b64 v[202:203], %[k2a] offset:5376\n\t" \
    "v_exp_f32 v111, v111\n\tv_exp_f32 v112, v112\n\tv_exp_f32 v113, v113\n\tv_exp_f32 v114, v114\n\tv_exp_f32 v115, v115\n\tv_exp_f32 v116, v116\n\t" \
    "v_exp_f32 v117, v117\n\tv_exp_f32 v118, v118\n\tv_smfmac_f32_32x32x32_bf16 v[128:143], v[220:223], v[104:111], v224\n\t" \
    "buffer_load_dwordx4 v[28:31], %[dv], %[rs], %[so] offen offset:2048 sc1\n\tv_exp_f32 v119, v119\n\tv_exp_f32 v120, v120\n\tv_exp_f32 v121, v121\n\t" \
    "v_exp_f32 v122, v122\n\tv_smfmac_f32_32x32x32_bf16 v[144:159], v[216:219], v[112:119], v224\n\t" \
    "buffer_load_dwordx4 v[32:35], %[dv], %[rs], %[so] offen offset:3072 sc1\n\ts_add_u32 %[so], %[so], 0x1000\n\tv_exp_f32 v123, v123\n\t" \
    "v_exp_f32 v124, v124\n\tv_exp_f32 v125, v125\n\tv_exp_f32 v126, v126\n\tv_exp_f32 v127, v127\n\ts_waitcnt lgkmcnt(0)\n\t" \
    "v_mfma_f32_32x32x16_bf16 v[96:111], v[204:207], v[192:195], v[160:175]\n\tv_exp_f32 v64, v64\n\tv_exp_f32 v65, v65\n\tv_exp_f32 v66, v66\n\t" \
    "v_smfmac_f32_32x32x32_bf16 v[144:159], v[220:223], v[120:127], v224\n\tds_read_b128 v[216:219], %[va] offset:6720\n\t" \
    "ds_read_b128 v[220:223], %[va] offset:7392\n\tv_exp_f32 v67, v67\n\tv_exp_f32 v68, v68\n\tv_exp_f32 v69, v69\n\tv_exp_f32 v70, v70\n\t" \
    "v_mfma_f32_32x32x16_bf16 v[112:127], v[204:207], v[196:199], v[176:191]\n\tds_read_b128 v[204:207], %[ka] offset:7680\n\t" \
    "buffer_load_dwordx4 v[36:39], %[dv], %[rs], %[so] offen offset:0 sc1\n\tv_exp_f32 v71, v71\n\tv_exp_f32 v72, v72\n\tv_exp_f32 v73, v73\n\t" \
    "v_exp_f32 v74, v74\n\tv_smfmac_f32_32x32x32_bf16 v[128:143], v[208:211], v[64:71], v224\n\t" \
    "buffer_load_dwordx4 v[40:43], %[dv], %[rs], %[so] offen offset:1024 sc1\n\tv_exp_f32 v75, v75\n\tv_exp_f32 v76, v76\n\tv_exp_f32 v77, v77\n\t" \
    "v_exp_f32 v78, v78\n\tv_mfma_f32_32x32x4_2b_bf16 v[96:127], v[202:203], v[200:201], v[96:127]\n\tds_read_b64 v[202:203], %[k2a] offset:7680\n\t" \
    "v_exp_f32 v79, v79\n\tv_exp_f32 v80, v80\n\tv_exp_f32 v81, v81\n\tv_exp_f32 v82, v82\n\tv_exp_f32 v83, v83\n\tv_exp_f32 v84, v84\n\t" \
    "v_exp_f32 v85, v85\n\tv_exp_f32 v86, v86\n\tv_smfmac_f32_32x32x32_bf16 v[128:143], v[212:215], v[72:79], v224\n\t" \
    "buffer_load_dwordx4 v[44:47], %[dv], %[rs], %[so] offen offset:2048 sc1\n\tv_exp_f32 v87, v87\n\tv_exp_f32 v88, v88\n\tv_exp_f32 v89, v89\n\t" \
    "v_exp_f32 v90, v90\n\tv_smfmac_f32_32x32x32_bf16 v[144:159], v[208:211], v[80:87], v224\n\t" \
    "buffer_load_dwordx4 v[48:51], %[dv], %[rs], %[so] offen offset:3072 sc1\n\ts_add_u32 %[so], %[so], 0x1000\n\tv_exp_f32 v91, v91\n\t" \
    "v_exp_f32 v92, v92\n\tv_exp_f32 v93, v93\n\tv_exp_f32 v94, v94\n\tv_exp_f32 v95, v95\n\ts_waitcnt lgkmcnt(0)\n\t" \
    "v_mfma_f32_32x32x16_bf16 v[64:79], v[204:207], v[192:195], v[160:175]\n\tv_exp_f32 v96, v96\n\tv_exp_f32 v97, v97\n\tv_exp_f32 v98, v98\n\t" \
    "v_smfmac_f32_32x32x32_bf16 v[144:159], v[212:215], v[88:95], v224\n\tds_read_b128 v[208:211], %[va] offset:8064\n\t" \
    "ds_read_b128 v[212:215], %[va] offset:8736\n\tv_exp_f32 v99, v99\n\tv_exp_f32 v100, v100\n\tv_exp_f32 v101, v101\n\tv_exp_f32 v102, v102\n\t" \
    "v_mfma_f32_32x32x16_bf16 v[80:95], v[204:207], v[196:199], v[176:191]\n\tds_read_b128 v[204:207], %[ka] offset:8704\n\tv_exp_f32 v103, v103\n\t" \
    "v_exp_f32 v104, v104\n\tv_exp_f32 v105, v105\n\tv_exp_f32 v106, v106\n\tv_smfmac_f32_32x32x32_bf16 v[128:143], v[216:219], v[96:103], v224\n\t" \
    "buffer_load_dwordx4 v[52:55], %[dv], %[rs], %[so] offen offset:0 sc1\n\tv_exp_f32 v107, v107\n\tv_exp_f32 v108, v108\n\tv_exp_f32 v109, v109\n\t" \
    "v_exp_f32 v110, v110\n\tv_mfma_f32_32x32x4_2b_bf16 v[64:95], v[202:203], v[200:201], v[64:95]\n\tds_read_b64 v[202:203], %[k2a] offset:7936\n\t" \
    "v_exp_f32 v111, v111\n\tv_exp_f32 v112, v112\n\tv_exp_f32 v113, v113\n\tv_exp_f32 v114, v114\n\tv_exp_f32 v115, v115\n\tv_exp_f32 v116, v116\n\t" \
    "v_exp_f32 v117, v117\n\tv_exp_f32 v118, v118\n\tv_smfmac_f32_32x32x32_bf16 v[128:143], v[220:223], v[104:111], v224\n\t" \
    "buffer_load_dwordx4 v[56:59], %[dv], %[rs], %[so] offen offset:1024 sc1\n\tv_exp_f32 v119, v119\n\tv_exp_f32 v120, v120\n\tv_exp_f32 v121, v121\n\t" \
    "v_exp_f32 v122, v122\n\tv_smfmac_f32_32x32x32_bf16 v[144:159], v[216:219], v[112:119], v224\n\t" \
    "buffer_load_dwordx4 v[60:63], %[dv], %[rs], %[so] offen offset:2048 sc1\n\tv_exp_f32 v123, v123\n\tv_exp_f32 v124, v124\n\tv_exp_f32 v125, v125\n\t" \
    "v_exp_f32 v126, v126\n\tv_exp_f32 v127, v127\n\t"
#define ASM_3C \
    "s_setprio 2\n\ts_waitcnt lgkmcnt(0)\n\tv_mfma_f32_32x32x16_bf16 v[96:111], v[204:207], v[192:195], v[160:175]\n\tv_exp_f32 v64, v64\n\t" \
    "v_exp_f32 v65, v65\n\tv_exp_f32 v66, v66\n\tv_smfmac_f32_32x32x32_bf16 v[144:159], v[220:223], v[120:127], v224\n\t" \
    "ds_read_b128 v[216:219], %[va] offset:1344\n\tds_read_b128 v[220:223], %[va] offset:2016\n\tv_exp_f32 v67, v67\n\tv_exp_f32 v68, v68\n\t" \
    "v_exp_f32 v69, v69\n\tv_exp_f32 v70, v70\n\tv_mfma_f32_32x32x16_bf16 v[112:127], v[204:207], v[196:199], v[176:191]\n\t" \
    "ds_read_b128 v[204:207], %[ka] offset:2560\n\tbuffer_load_dwordx4 %[dv], %[rs], %[so] offen offset:0 sc1 lds\n\tv_exp_f32 v71, v71\n\t" \
    "v_exp_f32 v72, v72\n\tv_exp_f32 v73, v73\n\tv_exp_f32 v74, v74\n\tv_smfmac_f32_32x32x32_bf16 v[128:143], v[208:211], v[64:71], v224\n\t" \
    "buffer_load_dwordx4 %[dv], %[rs], %[so] offen offset:1024 sc1 lds\n\tv_exp_f32 v75, v75\n\tv_exp_f32 v76, v76\n\tv_exp_f32 v77, v77\n\t" \
    "v_exp_f32 v78, v78\n\tv_mfma_f32_32x32x4_2b_bf16 v[96:127], v[202:203], v[200:201], v[96:127]\n\tds_read_b64 v[202:203], %[k2a] offset:2560\n\t" \
    "buffer_load_dwordx4 %[dv], %[rs], %[so] offen offset:2048 sc1 lds\n\tv_exp_f32 v79, v79\n\tv_exp_f32 v80, v80\n\tv_exp_f32 v81, v81\n\t" \
    "v_exp_f32 v82, v82\n\tv_exp_f32 v83, v83\n\tv_exp_f32 v84, v84\n\tv_exp_f32 v85, v85\n\tv_exp_f32 v86, v86\n\t" \
    "v_smfmac_f32_32x32x32_bf16 v[128:143], v[212:215], v[72:79], v224\n\tbuffer_load_dwordx4 %[dv], %[rs], %[so] offen offset:3072 sc1 lds\n\t" \
    "s_add_u32 %[so], %[so], 0x1000\n\ts_add_u32 m0, m0, 0x1000\n\tv_exp_f32 v87, v87\n\tv_exp_f32 v88, v88\n\tv_exp_f32 v89, v89\n\tv_exp_f32 v90, v90\n\t" \
    "v_smfmac_f32_32x32x32_bf16 v[144:159], v[208:211], v[80:87], v224\n\tbuffer_load_dwordx4 %[dv], %[rs], %[so] offen offset:0 sc1 lds\n\t" \
    "s_add_u32 %[so], %[so], 0x400\n\ts_add_u32 m0, m0, 0xffff4c00\n\tv_exp_f32 v91, v91\n\tv_exp_f32 v92, v92\n\tv_exp_f32 v93, v93\n\t" \
    "v_exp_f32 v94, v94\n\tv_exp_f32 v95, v95\n\ts_waitcnt lgkmcnt(0)\n\tv_mfma_f32_32x32x16_bf16 v[64:79], v[204:207], v[192:195], v[160:175]\n\t" \
    "v_exp_f32 v96, v96\n\tv_exp_f32 v97, v97\n\tv_exp_f32 v98, v98\n\tv_smfmac_f32_32x32x32_bf16 v[144:159], v[212:215], v[88:95], v224\n\t" \
    "ds_read_b128 v[208:211], %[va] offset:2688\n\tds_read_b128 v[212:215], %[va] offset:3360\n\tv_exp_f32 v99, v99\n\tv_exp_f32 v100, v100\n\t" \
    "v_exp_f32 v101, v101\n\tv_exp_f32 v102, v102\n\tv_mfma_f32_32x32x16_bf16 v[80:95], v[204:207], v[196:199], v[176:191]\n\t" \
    "ds_read_b128 v[204:207], %[ka] offset:3584\n\tbuffer_load_dwordx4 %[dv], %[rs], %[so] offen offset:0 sc1 lds\n\tv_exp_f32 v103, v103\n\t" \
    "v_exp_f32 v104, v104\n\tv_exp_f32 v105, v105\n\tv_exp_f32 v106, v106\n\tv_smfmac_f32_32x32x32_bf16 v[128:143], v[216:219], v[96:103], v224\n\t" \
    "buffer_load_dwordx4 %[dv], %[rs], %[so] offen offset:1024 sc1 lds\n\tv_exp_f32 v107, v107\n\tv_exp_f32 v108, v108\n\tv_exp_f32 v109, v109\n\t" \
    "v_exp_f32 v110, v110\n\tv_mfma_f32_32x32x4_2b_bf16 v[64:95], v[202:203], v[200:201], v[64:95]\n\tds_read_b64 v[202:203], %[k2a] offset:2816\n\t" \
    "buffer_load_dwordx4 %[dv], %[rs], %[so] offen offset:2048 sc1 lds\n\tv_exp_f32 v111, v111\n\tv_exp_f32 v112, v112\n\tv_exp_f32 v113, v113\n\t" \
    "v_exp_f32 v114, v114\n\tv_exp_f32 v115, v115\n\tv_exp_f32 v116, v116\n\tv_exp_f32 v117, v117\n\tv_exp_f32 v118, v118\n\t" \
    "v_smfmac_f32_32x32x32_bf16 v[128:143], v[220:223], v[104:111], v224\n\tbuffer_load_dwordx4 %[dv], %[rs], %[so] offen offset:3072 sc1 lds\n\t" \
    "s_add_u32 %[so], %[so], 0x1000\n\ts_add_u32 m0, m0, 0x1000\n\tv_exp_f32 v119, v119\n\tv_exp_f32 v120, v120\n\tv_exp_f32 v121, v121\n\t" \
    "v_exp_f32 v122, v122\n\tv_smfmac_f32_32x32x32_bf16 v[144:159], v[216:219], v[112:119], v224\n\t" \
    "buffer_load_dwordx4 %[dv], %[rs], %[so] offen offset:0 sc1 lds\n\tv_exp_f32 v123, v123\n\tv_exp_f32 v124, v124\n\tv_exp_f32 v125, v125\n\t" \
    "v_exp_f32 v126, v126\n\tv_exp_f32 v127, v127\n\t"
#define ASM_END1 \
    "s_setprio 3\n\tds_write_b128 %[xc], v[128:131] offset:0\n\tds_write_b128 %[xc], v[132:135] offset:1024\n\t" \
    "ds_write_b128 %[xc], v[136:139] offset:2048\n\tv_maximum3_f32 %[c0], |v128|, |v128|, |v129|\n\tv_maximum3_f32 %[c1], |v134|, |v134|, |v135|\n\t" \
    "v_maximum3_f32 %[c0], %[c0], |v130|, |v131|\n\tv_maximum3_f32 %[c1], %[c1], |v136|, |v137|\n\tv_maximum3_f32 %[c0], %[c0], |v132|, |v133|\n\t" \
    "v_maximum3_f32 %[c1], %[c1], |v138|, |v139|\n\ts_nop 1\n\tv_maximum3_f32 %[c0], %[c0], |v144|, |v145|\n\t" \
    "v_maximum3_f32 %[c1], %[c1], |v150|, |v151|\n\tv_maximum3_f32 %[c0], %[c0], |v146|, |v147|\n\tv_maximum3_f32 %[c1], %[c1], |v152|, |v153|\n\t" \
    "v_maximum3_f32 %[c0], %[c0], |v148|, |v149|\n\tv_maximum3_f32 %[c1], %[c1], |v154|, |v155|\n\tv_maximum3_f32 %[c0], %[c0], %[c1], %[c1]\n\t"
#define ASM_END0 \
    "s_setprio 3\n\tv_maximum3_f32 %[c0], |v128|, |v128|, |v129|\n\tv_maximum3_f32 %[c1], |v134|, |v134|, |v135|\n\t" \
    "v_maximum3_f32 %[c0], %[c0], |v130|, |v131|\n\tv_maximum3_f32 %[c1], %[c1], |v136|, |v137|\n\tv_maximum3_f32 %[c0], %[c0], |v132|, |v133|\n\t" \
    "v_maximum3_f32 %[c1], %[c1], |v138|, |v139|\n\ts_nop 4\n\tds_write_b128 %[xc], v[144:147] offset:0\n\tds_write_b128 %[xc], v[148:151] offset:1024\n\t" \
    "ds_write_b128 %[xc], v[152:155] offset:2048\n\tv_maximum3_f32 %[c0], %[c0], |v144|, |v145|\n\tv_maximum3_f32 %[c1], %[c1], |v150|, |v151|\n\t" \
    "v_maximum3_f32 %[c0], %[c0], |v146|, |v147|\n\tv_maximum3_f32 %[c1], %[c1], |v152|, |v153|\n\tv_maximum3_f32 %[c0], %[c0], |v148|, |v149|\n\t" \
    "v_maximum3_f32 %[c1], %[c1], |v154|, |v155|\n\tv_maximum3_f32 %[c0], %[c0], %[c1], %[c1]\n\t"
#define ASM_PRO \
    "ds_read_b128 v[216:219], %[ka]\n\tds_read_b128 v[192:195], %[qa]\n\tds_read_b128 v[196:199], %[qa] offset:1024\n\tds_read_b64 v[220:221], %[k2a]\n\t" \
    "ds_read_b64 v[200:201], %[q2a]\n\tds_read_b128 v[204:207], %[ka] offset:1024\n\tds_read_b64 v[202:203], %[k2a] offset:256\n\t" \
    "ds_read_b128 v[208:211], %[va]\n\tds_read_b128 v[212:215], %[va] offset:672\n\tv_mov_b32 v226, 1.0\n\tv_mov_b32 v227, 0\n\tv_mov_b32 v229, 0\n\t" \
    "v_mov_b32 v128, 0\n\tv_mov_b32 v129, 0\n\tv_mov_b32 v130, 0\n\tv_mov_b32 v131, 0\n\ts_waitcnt lgkmcnt(7)\n\t" \
    "v_mfma_f32_32x32x16_bf16 v[64:79], v[216:219], v[192:195], 0\n\ts_waitcnt lgkmcnt(6)\n\t" \
    "v_mfma_f32_32x32x16_bf16 v[80:95], v[216:219], v[196:199], 0\n\tv_mov_b32 v132, 0\n\tv_mov_b32 v133, 0\n\tv_mov_b32 v134, 0\n\tv_mov_b32 v135, 0\n\t" \
    "v_mov_b32 v136, 0\n\tv_mov_b32 v137, 0\n\tv_mov_b32 v138, 0\n\tv_mov_b32 v139, 0\n\tv_mov_b32 v140, 0\n\ts_waitcnt lgkmcnt(4)\n\t" \
    "v_mfma_f32_32x32x4_2b_bf16 v[64:95], v[220:221], v[200:201], v[64:95]\n\tv_mov_b32 v141, 0\n\tv_mov_b32 v142, 0\n\tv_mov_b32 v143, 0\n\t" \
    "v_mov_b32 v144, 0\n\tv_mov_b32 v145, 0\n\tv_mov_b32 v146, 0\n\tv_mov_b32 v147, 0\n\tv_mov_b32 v148, 0\n\tv_mov_b32 v149, 0\n\tv_mov_b32 v150, 0\n\t" \
    "v_mov_b32 v151, 0\n\tv_mov_b32 v152, 0\n\tv_mov_b32 v153, 0\n\tv_mov_b32 v154, 0\n\tv_mov_b32 v155, 0\n\tv_mov_b32 v156, 0\n\tv_mov_b32 v157, 0\n\t" \
    "v_mov_b32 v158, 0\n\tv_mov_b32 v159, 0\n\ts_nop 0\n\tv_max3_f32 v228, v64, v65, v66\n\tv_max3_f32 v230, v80, v81, v82\n\t" \
    "v_max3_f32 v228, v228, v67, v68\n\tv_max3_f32 v230, v230, v83, v84\n\tv_max3_f32 v228, v228, v69, v70\n\tv_max3_f32 v230, v230, v85, v86\n\t" \
    "v_max3_f32 v228, v228, v71, v72\n\tv_max3_f32 v230, v230, v87, v88\n\tv_max3_f32 v228, v228, v73, v74\n\tv_max3_f32 v230, v230, v89, v90\n\t" \
    "v_max3_f32 v228, v228, v75, v76\n\tv_max3_f32 v230, v230, v91, v92\n\tv_max3_f32 v228, v228, v77, v78\n\tv_max3_f32 v230, v230, v93, v94\n\t" \
    "v_max3_f32 v228, v228, v79, v79\n\tv_max3_f32 v230, v230, v95, v95\n\ts_nop 1\n\tv_permlane32_swap_b32 v228, v230\n\tv_max_f32 v228, v228, v230\n\t" \
    "v_fma_f32 v228, |v228|, %[up], -v228\n\tv_and_b32 v228, 0xffff0000, v228\n\ts_nop 1\n\t" \
    "v_mfma_f32_32x32x4_2b_bf16 v[160:191], v[226:227], v[228:229], 0\n\tv_mfma_f32_32x32x4_2b_bf16 v[64:95], v[226:227], v[228:229], v[64:95]\n\t" \
    "ds_write_b32 %[ma], v228\n\ts_nop 15\n\t"
#define ASM_TAIL2 \
    "s_setprio 0\n\ts_waitcnt lgkmcnt(0)\n\tv_mfma_f32_32x32x16_bf16 v[96:111], v[204:207], v[192:195], v[160:175]\n\tv_exp_f32 v64, v64\n\t" \
    "v_exp_f32 v65, v65\n\tv_exp_f32 v66, v66\n\tv_smfmac_f32_32x32x32_bf16 v[144:159], v[220:223], v[120:127], v224\n\t" \
    "ds_read_b128 v[216:219], %[va] offset:9408\n\tds_read_b128 v[220:223], %[va] offset:10080\n\tv_exp_f32 v67, v67\n\tv_exp_f32 v68, v68\n\t" \
    "v_exp_f32 v69, v69\n\tv_exp_f32 v70, v70\n\tv_mfma_f32_32x32x16_bf16 v[112:127], v[204:207], v[196:199], v[176:191]\n\tv_exp_f32 v71, v71\n\t" \
    "v_exp_f32 v72, v72\n\tv_exp_f32 v73, v73\n\tv_exp_f32 v74, v74\n\tv_smfmac_f32_32x32x32_bf16 v[128:143], v[208:211], v[64:71], v224\n\t" \
    "v_exp_f32 v75, v75\n\tv_exp_f32 v76, v76\n\tv_exp_f32 v77, v77\n\tv_exp_f32 v78, v78\n\ts_nop 0\n\t" \
    "v_mfma_f32_32x32x4_2b_bf16 v[96:127], v[202:203], v[200:201], v[96:127]\n\tv_exp_f32 v79, v79\n\tv_exp_f32 v80, v80\n\tv_exp_f32 v81, v81\n\t" \
    "v_exp_f32 v82, v82\n\tv_exp_f32 v83, v83\n\tv_exp_f32 v84, v84\n\tv_exp_f32 v85, v85\n\tv_exp_f32 v86, v86\n\t" \
    "v_smfmac_f32_32x32x32_bf16 v[128:143], v[212:215], v[72:79], v224\n\tv_exp_f32 v87, v87\n\tv_exp_f32 v88, v88\n\tv_exp_f32 v89, v89\n\t" \
    "v_exp_f32 v90, v90\n\tv_smfmac_f32_32x32x32_bf16 v[144:159], v[208:211], v[80:87], v224\n\tv_exp_f32 v91, v91\n\tv_exp_f32 v92, v92\n\t" \
    "v_exp_f32 v93, v93\n\tv_exp_f32 v94, v94\n\tv_exp_f32 v95, v95\n\ts_setprio 0\n\ts_waitcnt lgkmcnt(0)\n\tv_exp_f32 v96, v96\n\tv_exp_f32 v97, v97\n\t" \
    "v_exp_f32 v98, v98\n\tv_smfmac_f32_32x32x32_bf16 v[144:159], v[212:215], v[88:95], v224\n\ts_waitcnt vmcnt(15)\n\t" \
    "ds_read_b128 v[160:163], %[wp] offset:41984\n\tds_read_b128 v[164:167], %[wp] offset:43008\n\tds_read_b128 v[168:171], %[wp] offset:44032\n\t" \
    "ds_read_b128 v[172:175], %[wp] offset:45056\n\tds_read_b128 v[176:179], %[wp] offset:46080\n\tds_read_b128 v[180:183], %[wp] offset:0\n\t" \
    "ds_read_b128 v[184:187], %[wp] offset:1024\n\tds_read_b128 v[188:191], %[wp] offset:2048\n\tds_read_b128 v[208:211], %[wp] offset:3072\n\t" \
    "ds_read_b128 v[212:215], %[wp] offset:4096\n\tv_exp_f32 v99, v99\n\tv_exp_f32 v100, v100\n\tv_exp_f32 v101, v101\n\tv_exp_f32 v102, v102\n\t" \
    "buffer_load_dwordx4 v[64:67], %[dv], %[rs], %[oa] offen offset:0 sc1\n\tbuffer_load_dwordx4 v[68:71], %[dv], %[rs], %[oa] offen offset:1024 sc1\n\t" \
    "s_add_u32 %[oa], %[oa], 0xa000\n\tv_exp_f32 v103, v103\n\tv_exp_f32 v104, v104\n\tv_exp_f32 v105, v105\n\tv_exp_f32 v106, v106\n\t" \
    "v_smfmac_f32_32x32x32_bf16 v[128:143], v[216:219], v[96:103], v224\n\tbuffer_load_dwordx4 v[72:75], %[dv], %[rs], %[oa] offen offset:0 sc1\n\t" \
    "buffer_load_dwordx4 v[76:79], %[dv], %[rs], %[oa] offen offset:1024 sc1\n\ts_add_u32 %[oa], %[oa], 0xa000\n\tv_exp_f32 v107, v107\n\t" \
    "v_exp_f32 v108, v108\n\tv_exp_f32 v109, v109\n\tv_exp_f32 v110, v110\n\tv_exp_f32 v111, v111\n\tv_exp_f32 v112, v112\n\tv_exp_f32 v113, v113\n\t" \
    "v_exp_f32 v114, v114\n\tv_exp_f32 v115, v115\n\tv_exp_f32 v116, v116\n\tv_exp_f32 v117, v117\n\tv_exp_f32 v118, v118\n\t" \
    "v_smfmac_f32_32x32x32_bf16 v[128:143], v[220:223], v[104:111], v224\n\tbuffer_load_dwordx4 v[80:83], %[dv], %[rs], %[oa] offen offset:0 sc1\n\t" \
    "buffer_load_dwordx4 v[84:87], %[dv], %[rs], %[oa] offen offset:1024 sc1\n\ts_add_u32 %[oa], %[oa], 0xa000\n\tv_exp_f32 v119, v119\n\t" \
    "v_exp_f32 v120, v120\n\tv_exp_f32 v121, v121\n\tv_exp_f32 v122, v122\n\tv_smfmac_f32_32x32x32_bf16 v[144:159], v[216:219], v[112:119], v224\n\t" \
    "buffer_load_dwordx4 v[88:91], %[dv], %[rs], %[oa] offen offset:0 sc1\n\tbuffer_load_dwordx4 v[92:95], %[dv], %[rs], %[oa] offen offset:1024 sc1\n\t" \
    "v_exp_f32 v123, v123\n\tv_exp_f32 v124, v124\n\tv_exp_f32 v125, v125\n\tv_exp_f32 v126, v126\n\tv_exp_f32 v127, v127\n\ts_nop 1\n\t" \
    "v_smfmac_f32_32x32x32_bf16 v[144:159], v[220:223], v[120:127], v224\n\t"
#define STEPS(code, ...) asm volatile(code "s_setprio 3" : LOOP_STATE : LOOP_IN, [va] "v"(va) __VA_ARGS__ : LOOP_CLOB)
#define KADDR , [ka] "v"(ka), [k2a] "v"(k2a)
#define DV(i, k) [dv##i] "v"(kh ? V.o[(k) + i < 5 ? (k) + i : 4] : l16)
#define COPY(k) , DV(0, k), DV(1, k), DV(2, k), [dg] "s"(dsrc + (k) * 1024), "{m0}"(ddst + (k) * 1024)
#define LOOP_STATE "+{v[64:95]}"(SA), LOOP_REST
#define LOOP_REST LOOP_KV, "+{v[120:127]}"(PW)
#define LOOP_KV "+{v[128:143]}"(Oa), "+{v[144:159]}"(Ob), "+{v[202:203]}"(K2), "+{v[204:207]}"(K1), "+{v[208:211]}"(VE0), "+{v[212:215]}"(VE1), "+{v[220:223]}"(VO1)
#define LOOP_IN "{v[160:175]}"(Na), "{v[176:191]}"(Nb), LOOP_Q
#define LOOP_Q "{v[192:195]}"(Q1a), "{v[196:199]}"(Q1b), "{v[200:201]}"(Q2), "{v224}"(0xddddu)
#define LOOP_CLOB "v96", "v97", "v98", "v99", "v100", "v101", "v102", "v103", "v104", "v105", "v106", "v107", "v108", "v109", "v110", "v111", "v112", "v113", "v114", "v115", "v116", "v117", "v118", "v119", "v216", "v217", "v218", "v219", "scc", "memory"

struct Wreg { f32x32 a; f32x16 b; f32x8 c; f32x4 d; f32x8 r[4]; s16x8 p[10]; };
#define AREG(m) m "{v[64:71]}"(W.r[0]), m "{v[72:79]}"(W.r[1]), m "{v[80:87]}"(W.r[2]), m "{v[88:95]}"(W.r[3])
#define WREG(m) m "{v[4:35]}"(W.a), m "{v[36:51]}"(W.b), m "{v[52:59]}"(W.c), m "{v[60:63]}"(W.d)
#define WCOPY(code, outs) asm volatile(code "s_setprio 3" : LOOP_STATE, [so] "+s"(so) outs : LOOP_IN, [va] "v"(va) KADDR, [dv] "v"(l16), [rs] "s"(rs) VP : LOOP_CLOB)
struct Adr { unsigned ka, k2a, va, qa, q2a, ma; };
DI Adr p2_adr(unsigned lds0) {
    const int tid = threadIdx.x, lane = tid & 63, r = lane & 31, hh = lane >> 5;
    const int wid = __builtin_amdgcn_readfirstlane(tid >> 6), qg = wid & 3, kh = wid >> 2;
    const unsigned kr = (lane & 51) | ((lane & 4) << 1) | ((lane & 8) >> 1), r8 = (r & 15) * 16 + (r >> 4) * 8;
    Adr A = {lds0 + kh * 4 * QKB + kr * 16, lds0 + kh * 4 * QKB + 2048 + (kr & 15) * 16 + (r >> 4) * 8, lds0 + P2_V + kh * 4 * VBK + hh * 336 + (r < 20 ? r : 20) * 16,
             lds0 + STG + qg * QKB + lane * 16, lds0 + STG + qg * QKB + 2048 + hh * 256 + r8, lds0 + L_VSRC + tid * 4};
    asm volatile("" : "+v"(A.ka), "+v"(A.k2a), "+v"(A.va), "+v"(A.qa), "+v"(A.q2a), "+v"(A.ma));
    return A;
}
template <int kh>
DI void p2_body(char* ws, LDSP char* lds, rsrc_t rs, int h, int qb, int tile, u64 tag, const Vsrc& V, const Late& L, const Adr& A, Wreg& W STAMP_ARG) {
    const unsigned lds0 = (unsigned)(uintptr_t)lds;
    const int tid = threadIdx.x, lane = tid & 63, r = lane & 31, hh = lane >> 5;
    const int wid = __builtin_amdgcn_readfirstlane(tid >> 6), qg = wid & 3;
    const int tp = qb * 4 + qg;
    const unsigned l16 = lane * 16;
    const int rc = r < 20 ? r : 20;
    f32x16 Oa, Ob;
    s16x8 VO1;
    f32x8 PW;
    char* ob = ws + OFF_O + (h >> 2) * 5120;
    const unsigned oo = (tp * 4 + kh * 2 + (r >> 4)) * 40960 + (r & 15) * 16 + hh * 256;
    const char* dsrc; unsigned ddst;
    auto nextstage = [&](int st, unsigned buf) {
        if (!kh && wid == 0) glds16<true>(vsrc(h, 4, 0, lane), ws + OFF_VB + st * VST - 8192, lds0 + buf + 40960);
        dsrc = kh ? ws + OFF_VB + st * VST - 8192 : ws + OFF_KB + h * (32 * QKB) + st * (8 * QKB) + wid * 5120; ddst = lds0 + buf + wid * 5120;
    };
    const unsigned r8 = (r & 15) * 16 + (r >> 4) * 8;
    const unsigned va0 = P2_V + kh * 4 * VBK + hh * 336 + rc * 16;
    const unsigned sl = hh * 21 + rc, item = qg * 64 + lane;
    const LDSP char* kp = lds + kh * 4 * QKB;
    f32x16 Na, Nb;
    f32x32 SA;
    s16x8 Q1a, Q1b, K1, VE0, VE1;
    s16x4 Q2, K2;
    unsigned ka = A.ka, k2a = A.k2a, va = A.va;
    if (!kh) {
        if (!L.four) WAITV(0);
        WAITV_BAR(4);
        STAMP(8);
    }
    nextstage(1, 2 * STG);
    STAMP(9);
    asm volatile(ASM_PRO ASM_0A "s_setprio 3"
                 : "=&{v[64:95]}"(SA), "=&{v[128:143]}"(Oa), "=&{v[144:159]}"(Ob), "=&{v[202:203]}"(K2), "=&{v[204:207]}"(K1), "=&{v[208:211]}"(VE0), "=&{v[212:215]}"(VE1),
                   "=&{v[220:223]}"(VO1), "=&{v[120:127]}"(PW), "=&{v[160:175]}"(Na), "=&{v[176:191]}"(Nb), "=&{v[192:195]}"(Q1a), "=&{v[196:199]}"(Q1b), "=&{v[200:201]}"(Q2)
                 : "{v224}"(0xddddu), [va] "v"(va) KADDR COPY(0), [qa] "v"(A.qa), [q2a] "v"(A.q2a), [ma] "v"(A.ma), [up] "s"(1.f / 64) : "v226", "v227", "v228", "v229", "v230", LOOP_CLOB);
    if (tile < 240) {
        WAITV(2);
        arrive(lds, 1, 8, ws, FL_A + tile * FSTR, tag);
    }
#define CLEAR , [oo] "v"(oo), [em] "v"(u32x4{EMPTY, EMPTY, EMPTY, EMPTY}), [o1] "s"(ob + (h & 3) * 512), [o2] "s"(ob + 4096 + (h & 3) * 256)
    const unsigned fla = NEAR && lane >= 32 ? bflag(h, (lane & 7) + 8 + (lane & 24) * 2)
                       : FL_A + (lane < 12 ? ((lane >> 2) * 5 + ((20 * h + ((lane & 1) ? 19 : 0)) >> 7)) * 16 + 2 * qb + ((lane >> 1) & 1) : 240 + (lane & 15)) * FSTR;
    u64 pv;
    if (kh) STEPS(ASM_0B, KADDR COPY(2));
    else {
        f32x32 SB;
        s16x8 VO0;
#pragma unroll
        for (int i = 0; i < 8; ++i) SB[24 + i] = PW[i];
#define ODD(code, ...) asm volatile(code "s_setprio 3" : "+{v[64:95]}"(SA), LOOP_KV, "+{v[96:127]}"(SB), "+{v[216:219]}"(VO0) : LOOP_IN, [va] "v"(va) __VA_ARGS__ : "scc", "memory")
        ODD(ASM_0C, KADDR COPY(2));
        if (wid == 0) asm volatile("global_load_dwordx2 %0, %1, %2 sc1" : "=&v"(pv) : "v"(fla), "s"(ws) : "memory");
        ODD(ASM_0D, KADDR);
#pragma unroll
        for (int i = 0; i < 8; ++i) PW[i] = SB[24 + i];
    }
    s16x8 kA1, qA1, vA0, vA1, wf1; s16x4 kA2, qA2, wf2; f32x2 gg;
    auto loadA = [&]() {
        const unsigned aq = OFF_QA + (h * 32 + tp) * QKB, ak = aq + SEC_Q;
        const unsigned av = OFF_VA + (tp >> 3) * VST + ((tp & 7) * 2 + kh) * (4 * VGS) + h * 320;
        const unsigned ones = OFF_VB + ((20 * h) >> 7) * VGS + 10240 - av, vo = hh * VGS + r * 16;
        kA1 = dl16(rs, l16, ak + kh * 1024);
        kA2 = __builtin_bit_cast(s16x4, dl8(rs, r8, ak + 2048 + kh * 256));
        wf1 = dl16(rs, l16, OFF_WPF + h * 1536);
        wf2 = __builtin_bit_cast(s16x4, dl8(rs, lane * 8, OFF_WPF + h * 1536 + 1024));
        qA1 = dl16(rs, l16, aq + kh * 1024);
        qA2 = __builtin_bit_cast(s16x4, dl8(rs, r8, aq + 2048 + kh * 256));
        vA0 = dl16(rs, r < 20 ? vo : ones, av);
        vA1 = dl16(rs, r < 20 ? vo + 2 * VGS : ones, av);
        gg = __builtin_bit_cast(f32x2, dl8(rs, r * 8, OFF_G + (tp * 64 + kh * 32) * 8));
    };
    f32x8 pq0, pq1;
    u32x2 pq2;
    float sq;
    auto branchA = [&]() {
        asm volatile("" : "+v"(kA1), "+v"(qA1), "+v"(kA2), "+v"(qA2), "+v"(vA0), "+v"(vA1), "+v"(wf1), "+v"(wf2), "+v"(gg));
        f32x2 e[10];
        float sp, sn;
        features(kA1, kA2, wf1, wf2, e, sp, sn);
        sp = __builtin_amdgcn_rcpf(sp); sn = __builtin_amdgcn_rcpf(sn);
        LDSP char* img = lds + P2_SCR + wid * 3360;
#pragma unroll
        for (int i = 0; i < 10; ++i) e[i] *= i < 5 ? sp : sn;
#pragma unroll
        for (int i = 0; i < 5; ++i) *(LDSP u32x2*)(img + r * 80 + 16 * i + 8 * hh) = u32x2{cvtpk(e[2 * i][0], e[2 * i][1]), cvtpk(e[2 * i + 1][0], e[2 * i + 1][1])};
        features(qA1, qA2, wf1, wf2, e, sq, sn);
        sn = sq * __builtin_amdgcn_rcpf(sn);
#pragma unroll
        for (int i = 5; i < 10; ++i) e[i] *= sn;
#pragma unroll
        for (int i = 0; i < 8; ++i) { pq0[i] = e[i >> 1][i & 1]; pq1[i] = e[4 + (i >> 1)][i & 1]; }
        pq2 = u32x2{__builtin_amdgcn_perm(__float_as_uint(e[8][1]), __float_as_uint(e[8][0]), 0x07060302u), __builtin_amdgcn_perm(__float_as_uint(e[9][1]), __float_as_uint(e[9][0]), 0x07060302u)};
        sq *= 1e-6f;
        asm volatile("" : "+v"(pq0), "+v"(pq1), "+v"(pq2), "+v"(sq));
        const LDSP char* t0 = img + (4 * hh + ((lane & 15) >> 2)) * 80 + ((lane >> 4) & 1) * 32 + (lane & 3) * 8;
        const LDSP char* t1 = img + (4 * hh + ((lane & 15) >> 2)) * 80 + 64 + (lane & 1) * 8;
        f32x16 d0 = MFMA16(cat8(trread(t0), trread(t0 + 640)), vA0, f32x16{});
        f32x16 d1 = MFMA16(cat8(trread(t1), trread(t1 + 640)), vA0, f32x16{});
        d0 = MFMA16(cat8(trread(t0 + 1280), trread(t0 + 1920)), vA1, d0);
        d1 = MFMA16(cat8(trread(t1 + 1280), trread(t1 + 1920)), vA1, d1);
#pragma unroll
        for (int i = 0; i < 4; ++i) *(LDSP f32x4*)(img + sl * 16 + i * 672) = f32x4{d0[4 * i], d0[4 * i + 1], d0[4 * i + 2], d0[4 * i + 3]};
        *(LDSP f32x4*)(img + sl * 16 + 4 * 672) = f32x4{d1[0], d1[1], d1[2], d1[3]};
    };
    f32x4 part[8];
    u32x2 outA4;
    float rm;
    unsigned xc, xa, wp;
    auto finishA = [&]() {
        const LDSP char* kf = lds + P2_KVF + (hh * 42 + rc) * 8;
        f32x16 nm = MFMA8(ld4(lds + P2_KVF + sl * 8 + 1344), __builtin_bit_cast(s16x4, pq2), f32x16{});
        nm = SMFMA(cat8(ld4(kf), ld4(kf + 168)), pq0, nm);
        nm = SMFMA(cat8(ld4(kf + 672), ld4(kf + 840)), pq1, nm);
        auto dn = __builtin_amdgcn_permlane32_swap(__float_as_uint(nm[8]), __float_as_uint(nm[8]), false, false);
        const float sa = gg[0] * __builtin_amdgcn_rcpf(__uint_as_float(dn[1]) + sq);
        st16(ob + (h & 3) * 512, oo, u32x4{cvto(nm[0] * sa, nm[1] * sa), cvto(nm[2] * sa, nm[3] * sa), cvto(nm[4] * sa, nm[5] * sa), cvto(nm[6] * sa, nm[7] * sa)});
        outA4 = u32x2{cvto(nm[8] * sa, nm[9] * sa), cvto(nm[10] * sa, nm[11] * sa)};
        PIN(outA4);
        const float dm = (kh ? Nb[0] : Na[0]) - *(const LDSP float*)(lds + L_VSRC + ((wid ^ 4) * 64 + kh * 32 + r) * 4);
        if (!(fabsf(dm) <= 50.f)) *(LDSP unsigned*)(lds + L_WORD) = 1u;
        rm = ex2(dm);
        xc = lds0 + L_XCH + wid * 3072 + l16; xa = lds0 + L_XCH + (wid ^ 4) * 3072 + l16; wp = lds0 + STG + wid * 5120 + l16;
        asm volatile("" : "+v"(rm), "+v"(xc), "+v"(xa), "+v"(wp));
    };
    const unsigned wo = OFF_WOF + ((blockIdx.x >> 5) * 8 + wid) * 25600;
    unsigned so3;
    u64 fa[4];
    static_assert(FSTR == 128, "offsets of the four flags");
#pragma unroll
    for (int n = 1; n < 4; ++n) {
        STAMP(8 + 2 * n);
        if (!kh && n == 1 && wid == 0) {
            asm volatile("s_waitcnt vmcnt(0)" : "+v"(pv) :: "memory");
            if (__builtin_expect(!allset(pv, tag), 0)) waitflags(ws, fla, tag);
        }
        if (!kh && n == 3 && wid == 0) waitflags(ws, FL_KV + (lane < 32 ? h * 8 + (lane & 7) : (lane & 31) * 8 + (blockIdx.x & 7)) * FSTR, tag);
        const bool last = !kh && n >= 2 && blockIdx.x == 0 && wid == 1;
        if (n == 3 && __builtin_expect(last, 0)) {
            asm volatile("s_waitcnt vmcnt(0)" : "+v"(fa[0]), "+v"(fa[1]), "+v"(fa[2]), "+v"(fa[3]) :: "memory");
            if (!(allset(fa[0], tag) && allset(fa[1], tag) && allset(fa[2], tag) && allset(fa[3], tag)))
#pragma nounroll
                for (int i = 0; i < 4; ++i) waitflags(ws, FL_A + (lane * 4 + i) * FSTR, tag);
        }
        WAITV_BAR(0);
        if (n == 2 && __builtin_expect(last, 0))
            asm volatile("global_load_dwordx2 %0, %4, %5 sc1\n\tglobal_load_dwordx2 %1, %4, %5 offset:128 sc1\n\tglobal_load_dwordx2 %2, %4, %5 offset:256 sc1\n\t"
                         "global_load_dwordx2 %3, %4, %5 offset:384 sc1" : "=&v"(fa[0]), "=&v"(fa[1]), "=&v"(fa[2]), "=&v"(fa[3]) : "v"(FL_A + lane * 4 * FSTR), "s"(ws) : "memory");
        if (n == 3 && __builtin_expect(last, 0)) setflag(ws, OFF_EPO, tag);
        STAMP(9 + 2 * n);
        if (kh && n == 1) { loadA(); SBAR(); }
        if (kh) {
            if (n == 2) {
                const LDSP char* sc = lds + P2_SCR + (item < 216 ? item : 0) * 16;
                f32x4 s = *(const LDSP f32x4*)sc;
#pragma unroll
                for (int i = 1; i < 8; ++i) s += *(const LDSP f32x4*)(sc + i * 3360);
                if (item < 216) st16(ws + OFF_KVP + (h * 8 + qb) * KVP + qg * 1024, l16, __builtin_bit_cast(u32x4, s));
            }
        } else if (n == 3) {
#pragma unroll
            for (int i = 0; i < 8; ++i) part[i] = __builtin_bit_cast(f32x4, dl16(rs, l16, OFF_KVP + (h * 8 + i) * KVP + qg * 1024));
        }
        if (n == TOUCHN) {
            const unsigned ln = ((blockIdx.x >> 3) * 8 + wid) * 50 + (lane < 50 ? lane : 0);
            asm volatile("s_nop 0\n\tglobal_load_lds_dword %0, %1 sc1" :: "v"(ln * 128), "s"(ws + OFF_WOF), "{m0}"(lds0 + L_WORD + 64 + wid * 256) : "memory");
        }
        STAMPT(n, 24);
        const int step = n == 1 ? 2 * (int)STG : -(int)STG;
        const unsigned vp = va;
        ka += step; k2a += step; va += step;
        if (n < 3) nextstage(n + 1, n == 1 ? STG : 0);
        if (kh && n == 1) STEPS(ASM_1G, KADDR, [vp] "v"(vp));
        else if (n == 1) STEPS(ASM_1HC, KADDR COPY(0) CLEAR, [vp] "v"(vp));
        else if (n == 2) STEPS(ASM_1H, KADDR COPY(0), [vp] "v"(vp));
        else if (!kh) STEPS(ASM_3H, KADDR, [vp] "v"(vp));
        else {
            unsigned so = wo, m = lds0 + 2 * STG + wid * 5120;
#define VP , [vp] "v"(vp)
            WCOPY(ASM_3G, COMMA "+{m0}"(m));
#undef VP
#define VP
        }
        STAMPT(n, 25);
        if (n == 1) { if (kh) { if (F1 == 2) branchA(); } else { loadA(); SBAR(); } }
        if (kh && n == 2) { WAITV(2); arrive(lds, 2, 4, ws, FL_KV + (h * 8 + qb) * FSTR, tag); }
        if (n == 3) {
            if (!kh) {
                const f32x4 s = ((part[0] + part[1]) + (part[2] + part[3])) + ((part[4] + part[5]) + (part[6] + part[7]));
                *(LDSP u32x2*)(lds + P2_KVF + item * 8) = u32x2{cvtpk(s[0], s[1]), cvtpk(s[2], s[3])};
            }
            asm volatile("s_waitcnt lgkmcnt(0)\n\ts_barrier" ::: "memory");
            if (!kh) finishA();
            unsigned so = wo + kh * 5120, m = lds0 + (2 - kh) * STG + wid * 5120;
            STAMPT(n, 26);
            if (kh) WCOPY(ASM_3A, COMMA "+{m0}"(m)); else WCOPY(ASM_3C, COMMA "+{m0}"(m));
            STAMPT(n, 27);
            if (kh) finishA();
            STAMPT(n, 28);
            so = wo + 10240;
            WCOPY(ASM_3B, COMMA WREG("=&"));
            so3 = so;
        } else if (n == 2) {
            STAMPT(n, 26);
            STEPS(ASM_1A, KADDR COPY(2));
            STEPS(ASM_1B, KADDR COPY(4));
        } else if (kh) {
            STAMPT(n, 26);
            STEPS(ASM_1AC, KADDR COPY(0) CLEAR);
            if (F1 == 4) branchA();
            STEPS(ASM_1C, KADDR COPY(2));
        } else {
            STAMPT(n, 26);
            STEPS(ASM_1A, KADDR COPY(2));
            if (F0 == 4) branchA();
            STEPS(ASM_1D, KADDR COPY(4));
            STAMPT(n, 27);
            if (F0 == 6) branchA();
            STAMPT(n, 28);
            STEPS(ASM_1E, KADDR);
            if (F0 == 8) branchA();
        }
    }
    if (kh && qg == 3) {
        const unsigned ln = (blockIdx.x >> 3) * 64 + lane;
        asm volatile("s_nop 0\n\tglobal_load_lds_dword %0, %1 sc1" :: "v"((ln >> 7) * 40960 + ((ln >> 4) & 7) * 5120 + (ln & 15) * 128), "s"(ws + OFF_O + (blockIdx.x & 7) * 16 * 40960), "{m0}"(lds0 + L_WORD + 64) : "memory");
    }
    {
        unsigned oa = OFF_O + ((blockIdx.x & 7) * 4 + ((blockIdx.x >> 3) & 3)) * 4 * 40960 + wid * 5120;
        float c0, c1;
        static_assert(STG == 41984, "offsets in the tail");
#define TAIL(end) asm volatile(ASM_TAIL2 end : LOOP_REST, [so] "+s"(so3), [oa] "+s"(oa), WREG("+"), AREG("="), [c0] "=&v"(c0), [c1] "=&v"(c1), "+{v[160:175]}"(Na), "+{v[176:191]}"(Nb) \
                               : LOOP_Q, "{v[64:95]}"(SA), [va] "v"(va), [dv] "v"(l16), [rs] "s"(rs), [xc] "v"(xc), [wp] "v"(wp) : LOOP_CLOB)
        if (kh) TAIL(ASM_END1); else TAIL(ASM_END0);
        if (!(c0 < 1.0e21f)) *(LDSP unsigned*)(lds + L_WORD) = 1u;
    }
    STAMP(16);
    f32x4 xp[3];
    unsigned over;
    asm volatile("s_waitcnt vmcnt(8) lgkmcnt(0)\n\ts_barrier\n\tds_read_b32 %[f], %[fa]\n\tds_read_b128 %[c], %[xa] offset:2048\n\tds_read_b128 %[a], %[xa]\n\t"
                 "ds_read_b128 %[b], %[xa] offset:1024\n\ts_waitcnt lgkmcnt(0)"
                 : WREG("+"), AREG("+"), "+{v[160:175]}"(Na), "+{v[176:191]}"(Nb), "+{v[208:211]}"(VE0), "+{v[212:215]}"(VE1),
                   [f] "=&v"(over), [a] "=&v"(xp[0]), [b] "=&v"(xp[1]), [c] "=&v"(xp[2]) : [fa] "v"(lds0 + L_WORD), [xa] "v"(xa) : "memory");
#pragma unroll
    for (int k = 0; k < 8; ++k) {
        const f32x16& n = k < 4 ? Na : Nb;
        W.p[k] = __builtin_bit_cast(s16x8, f32x4{n[4 * (k & 3)], n[4 * (k & 3) + 1], n[4 * (k & 3) + 2], n[4 * (k & 3) + 3]});
    }
    W.p[8] = VE0; W.p[9] = VE1;
    if (__builtin_expect(__builtin_amdgcn_readfirstlane(over) != 0u, 0)) {
        asm volatile("s_waitcnt vmcnt(0)" : AREG("+") :: "memory");
        const Vsrc F = stage_lanes(h, wid, lane);
        auto issue = [&](int st) {
            if (wid == 0) glds16<true>(vsrc(h, 4, 0, lane), ws + OFF_VB + st * VST - 8192, lds0 + 40960);
            stage_all(ws, h, st, wid, lane, F, lds0);
        };
        float ma = -3.0e38f, mb = -3.0e38f;
        rm = 1.f;
#pragma nounroll
        for (int st = 0; st < 4; ++st) {
            issue(st);
            WAITV_BAR(0);
#pragma nounroll
            for (int j = 0; j < 16; ++j) {
                const LDSP char* kb = lds + (j >> 1) * QKB;
                const s16x8 k1 = ld8(kb + (j & 1) * 1024 + l16);
                rowmax2(MFMA4B(ld4(kb + 2048 + (j & 1) * 256 + r8), Q2, cat32(MFMA16(k1, Q1a, f32x16{}), MFMA16(k1, Q1b, f32x16{}))), ma, mb);
            }
            WAITV_BAR(0);
        }
        RED2(ma, mb, fmaxf);
#pragma unroll
        for (int i = 0; i < 16; ++i) { Na[i] = -ma; Nb[i] = -mb; }
        Oa = f32x16{}; Ob = f32x16{};
#pragma nounroll
        for (int st = 0; st < 4; ++st) {
            issue(st);
            WAITV_BAR(0);
#pragma nounroll
            for (int j = 0; j < 8; ++j) {
                const LDSP char* kb = kp + (j >> 1) * QKB;
                const LDSP char* vb = lds + va0 + (j >> 1) * VBK + (j & 1) * 1344;
                const s16x8 k1 = ld8(kb + (j & 1) * 1024 + l16), v0 = ld8(vb), v1 = ld8(vb + 672);
                f32x32 s = MFMA4B(ld4(kb + 2048 + (j & 1) * 256 + r8), Q2, cat32(MFMA16(k1, Q1a, Na), MFMA16(k1, Q1b, Nb)));
                u32x4 p[4];
#pragma unroll
                for (int i = 0; i < 16; ++i) p[i >> 2][i & 3] = cvtpk(ex2(s[2 * i]), ex2(s[2 * i + 1]));
                Oa = MFMA16(v0, __builtin_bit_cast(s16x8, p[0]), Oa); Oa = MFMA16(v1, __builtin_bit_cast(s16x8, p[1]), Oa);
                Ob = MFMA16(v0, __builtin_bit_cast(s16x8, p[2]), Ob); Ob = MFMA16(v1, __builtin_bit_cast(s16x8, p[3]), Ob);
            }
            WAITV_BAR(0);
        }
        const f32x16 X = kh ? Oa : Ob;
#pragma unroll
        for (int i = 0; i < 3; ++i) *(LDSP f32x4*)(lds + L_XCH + wid * 3072 + l16 + i * 1024) = f32x4{X[4 * i], X[4 * i + 1], X[4 * i + 2], X[4 * i + 3]};
        WAITV_BAR(0);
#pragma unroll
        for (int i = 0; i < 3; ++i) xp[i] = *(const LDSP f32x4*)(lds + L_XCH + (wid ^ 4) * 3072 + l16 + i * 1024);
#pragma unroll
        for (int k = 0; k < 10; ++k) W.p[k] = ld8(lds + (k < 5 ? 2 * STG : STG) + wid * 5120 + (k % 5) * 1024 + l16);
    }
    STAMP(17);
    float O[12];
#pragma unroll
    for (int i = 0; i < 12; ++i) O[i] = __builtin_fmaf(xp[i >> 2][i & 3], rm, kh ? Ob[i] : Oa[i]);
    auto ls = __builtin_amdgcn_permlane32_swap(__float_as_uint(O[8]), __float_as_uint(O[8]), false, false);
    const float sb = gg[1] * __builtin_amdgcn_rcpf(__uint_as_float(ls[1]));
    st16(ob + 2048 + (h & 3) * 512, oo, u32x4{cvto(O[0] * sb, O[1] * sb), cvto(O[2] * sb, O[3] * sb), cvto(O[4] * sb, O[5] * sb), cvto(O[6] * sb, O[7] * sb)});
    st16m(ob + 4096 + (h & 3) * 256, oo, u32x4{outA4[0], outA4[1], cvto(O[8] * sb, O[9] * sb), cvto(O[10] * sb, O[11] * sb)}, 0xffffffffull);
    if (XT != 5) STAMP(18);
}

#define P3_EARLY 0
#define P3_SC1 0
#define BARP 1
#define LSCH 0x5431
#define CT 1
#define CTW 4
#define ROTK 0
#define RK 3
#define LK0 8
#define PD3 6
constexpr int lsch(int j) { return (LSCH >> (4 * j)) & 15; }
constexpr int asked(int p) { int n = 0; for (int j = 0; j < 4; ++j) n += lsch(j) < p; return n; }
#define P3_PRIO(x) asm volatile("s_setprio %0" :: "n"(x))
DI void p3_body(char* ws, char* out, LDSP char* smem, rsrc_t rs, int h, int qb, u64 tag, Wreg& W STAMP_ARG) {
    const int tid = threadIdx.x, lane = tid & 63;
    const int wid = __builtin_amdgcn_readfirstlane(tid >> 6);
    const int b = blockIdx.x, grp = b & 7, tt = grp * 4 + ((b >> 3) & 3), nq = b >> 5;
    const unsigned ob = OFF_O + tt * 4 * 40960 + wid * 5120;
    const unsigned l16 = lane * 16;
    s16x8 af[5][5], bf[4][5];
    f32x4 acc[4][5];
    auto ldo = [&](int j, int k0, int k1) {
#pragma unroll
        for (int ks = k0; ks < k1; ++ks) bf[j][ks] = dl16(rs, l16 + (ks & 3) * 1024, ob + j * 40960 + (ks >> 2) * 4096);
        SBAR();
    };
    auto ask = [&](int p) {
#pragma unroll
        for (int j = 0; j < 4; ++j) if (lsch(j) == p) ldo(j, 2, 5);
    };
    auto missing = [&](int j, int k0, int k1) {
        unsigned m = 0;
#pragma unroll
        for (int ks = k0; ks < k1; ++ks) {
            const u32x4 v = __builtin_bit_cast(u32x4, bf[j][ks]);
            m = max(max(max(m, v[0]), v[1]), max(v[2], v[3]));
        }
        return __builtin_amdgcn_ballot_w64(m == EMPTY) != 0;
    };
    auto again = [&](int j, int k0, int k1, int end) {
        int it = 0;
#pragma nounroll
        do {
            __builtin_amdgcn_s_sleep(SLP);
            asm volatile("" ::: "memory");
#pragma unroll
            for (int jj = j; jj < end; ++jj) ldo(jj, k0, k1);
        } while (missing(j, k0, k1) && ++it < 4096);
    };
    unsigned mx = 0;
    bool bad;
    auto look = [&](auto pc, auto sc) {
        constexpr int p = decltype(pc)::v, st = decltype(sc)::v, n = p < 4 ? 4 : 6, j = p & 3;
        if constexpr (p < 8 && st >= 0 && st <= n) {
            if constexpr (p < 4 && st == 0) {
                constexpr int young = 2 * (3 - j) + 2 + 3 * asked(j - 1);
                if (j == 0) asm volatile("s_waitcnt vmcnt(%1)" : "+{v[64:71]}"(W.r[0]) : "n"(young) : "memory");
                if (j == 1) asm volatile("s_waitcnt vmcnt(%1)" : "+{v[72:79]}"(W.r[1]) : "n"(young) : "memory");
                if (j == 2) asm volatile("s_waitcnt vmcnt(%1)" : "+{v[80:87]}"(W.r[2]) : "n"(young) : "memory");
                if (j == 3) asm volatile("s_waitcnt vmcnt(%1)" : "+{v[88:95]}"(W.r[3]) : "n"(young) : "memory");
                bf[j][0] = __builtin_bit_cast(s16x8, __builtin_shufflevector(W.r[j], W.r[j], 0, 1, 2, 3));
                bf[j][1] = __builtin_bit_cast(s16x8, __builtin_shufflevector(W.r[j], W.r[j], 4, 5, 6, 7));
            }
            auto word = [&](int i) { i = i < 2 * n ? i : 2 * n - 1; return __builtin_bit_cast(u32x4, bf[j][(p < 4 ? 0 : 2) + i / 4])[i % 4]; };
            if (st == n) bad = __builtin_amdgcn_ballot_w64(mx == EMPTY) != 0;
            else if (st) asm volatile("v_max3_u32 %0, %0, %1, %2" : "+v"(mx) : "v"(word(2 * st)), "v"(word(2 * st + 1)));
            else asm volatile("v_max_u32 %0, %1, %2" : "+v"(mx) : "v"(word(0)), "v"(word(1)));
            SBAR();
        }
    };
#pragma unroll
    for (int k = 0; k < 10; ++k) af[k / 5][k % 5] = W.p[k];
#pragma unroll
    for (int k = 0; k < 15; ++k) {
        f32x4 v;
#pragma unroll
        for (int i = 0; i < 4; ++i) v[i] = k < 8 ? W.a[(4 * k + i) & 31] : k < 12 ? W.b[(4 * k + i) & 15] : k < 14 ? W.c[(4 * k + i) & 7] : W.d[i];
        af[2 + k / 5][k % 5] = __builtin_bit_cast(s16x8, v);
    }
    STAMP3(24);
    P3_PRIO(1);
    if (XT != 5) STAMP(19);
    sfor<0, 5>([&](auto sc) __attribute__((always_inline)) { look(IC<0>{}, sc); });
    sfor<0, 4>([&](auto jc) __attribute__((always_inline)) {
        constexpr int j = decltype(jc)::v;
        if (__builtin_expect(bad, 0)) again(j, 0, 2, j + 1);
        if (j == 0) STAMP3(25);
        if (j == 3) STAMP3(26);
        sfor<0, 10>([&](auto kc) __attribute__((always_inline)) {
            constexpr int k = decltype(kc)::v;
            acc[j][k % 5] = MFMAS(bf[j][k / 5], af[k / 5][k % 5], k / 5 ? acc[j][k % 5] : f32x4{});
            SBAR();
            look(IC<j + 1>{}, IC<k - (j < 3 ? 4 : 2)>{});
        });
        ask(j);
        if (j == BARP) asm volatile("s_waitcnt lgkmcnt(0)\n\ts_barrier" ::: "memory");
    });
    P3_PRIO(0);
    if (XT != 5) STAMP(20);
    const unsigned m0a = (unsigned)(uintptr_t)smem + wid * 1024, m0b = m0a + 7 * 8192;
    const unsigned far = m0a + 15 * 8192 + lane * 4;
    auto put = [&](auto tc, auto cc) {
        constexpr int t = decltype(tc)::v, c = decltype(cc)::v, j = t / 5, i = t % 5;
        if constexpr (t < 0 || t >= 20) return;
        else if constexpr (t == 0 && c == 0) asm volatile("s_nop 3\n\tds_write_b32 %1, %0" :: "v"(acc[j][i][c]), "v"(far) : "memory");
        else if constexpr (j == 0) asm volatile("ds_write_b32 %2, %0 offset:%1" :: "v"(acc[j][i][c]), "n"(i * 8192 + c * 256), "v"(far) : "memory");
        else if constexpr (c == 0 && (t == 5 || t == 13))
            asm volatile("s_nop 0\n\tds_write_addtid_b32 %0 offset:%1" :: "v"(acc[j][i][c]), "n"((t < 13 ? t - 5 : t - 12) * 8192 + c * 256), "{m0}"(t < 13 ? m0a : m0b) : "memory");
        else asm volatile("ds_write_addtid_b32 %0 offset:%1" :: "v"(acc[j][i][c]), "n"((t < 13 ? t - 5 : t - 12) * 8192 + c * 256), "{m0}"(t < 13 ? m0a : m0b) : "memory");
        SBAR();
    };
    auto puts = [&](auto gc) {
        constexpr int g = decltype(gc)::v - PD3, t = g >= 0 ? g / 3 : -1;
        if constexpr (g % 3 == 0) { put(IC<t>{}, IC<0>{}); put(IC<t>{}, IC<1>{}); }
        if constexpr (g % 3 == 1) put(IC<t>{}, IC<2>{});
        if constexpr (g % 3 == 2) put(IC<t>{}, IC<3>{});
    };
    sfor<0, 4>([&](auto jc) __attribute__((always_inline)) {
        constexpr int j = decltype(jc)::v;
        if (__builtin_expect(bad, 0)) again(j, 2, 5, asked(4 + j));
        if (j == 0) STAMP3(27);
        if (j == 3) STAMP3(28);
        sfor<0, 15>([&](auto kc) __attribute__((always_inline)) {
            constexpr int k = decltype(kc)::v, i = k / 3, ks = 2 + k % 3;
            acc[j][i] = MFMAS(bf[j][ks], af[ks][i], acc[j][i]);
            SBAR();
            puts(IC<15 * j + k>{});
            look(IC<j + 5>{}, IC<k - LK0>{});
        });
        ask(4 + j);
    });
    sfor<60, 57 + PD3>([&](auto gc) __attribute__((always_inline)) { puts(gc); });
    asm volatile("s_nop %0" :: "n"(PD3 == 6 ? 3 : 5));
    sfor<57 + PD3, 60 + PD3>([&](auto gc) __attribute__((always_inline)) { puts(gc); });
    unsigned oo[3];
#pragma unroll
    for (int k = 0; k < 3; ++k) {
        const int T = wid + 8 * k, j = (T * 13) >> 6;
        oo[k] = ((lane & 12) + (lane >> 4)) * 1280 + (lane & 3) * 8 + ((tt * 64 + ((j + 1) & 3) * 16) * 1280 + (nq * 80 + (T - 5 * j) * 16) * 2);
    }
    unsigned la[3];
#pragma unroll
    for (int k = 0; k < 3; ++k) la[k] = (unsigned)(uintptr_t)smem + k * 65536 + wid * 8192 + lane * 16;
    asm volatile("" : "+v"(oo[0]), "+v"(oo[1]), "+v"(oo[2]), "+v"(la[0]), "+v"(la[1]), "+v"(la[2]));
    if (XT != 5) STAMP(21);
    asm volatile("s_waitcnt lgkmcnt(0)\n\ts_barrier" ::: "memory");
    if (XT != 5) STAMP(22);
    f32x4 q[3][8];
#define Q8(m, k) m(q[k][0]), m(q[k][1]), m(q[k][2]), m(q[k][3]), m(q[k][4]), m(q[k][5]), m(q[k][6]), m(q[k][7])
    auto get = [&](int k) {
        asm volatile("ds_read_b128 %0, %8\n\tds_read_b128 %1, %8 offset:1024\n\tds_read_b128 %2, %8 offset:2048\n\tds_read_b128 %3, %8 offset:3072\n\t"
                     "ds_read_b128 %4, %8 offset:4096\n\tds_read_b128 %5, %8 offset:5120\n\tds_read_b128 %6, %8 offset:6144\n\tds_read_b128 %7, %8 offset:7168"
                     : Q8("=&v", k) : "v"(la[k]) : "memory");
    };
    auto sum = [&](int k, auto nc) {
        asm volatile("s_waitcnt lgkmcnt(%8)" : Q8("+v", k) : "n"(decltype(nc)::v) : "memory");
        const f32x4 s = ((q[k][0] + q[k][1]) + (q[k][2] + q[k][3])) + ((q[k][4] + q[k][5]) + (q[k][6] + q[k][7]));
        *(GLBP u32x2*)((GLBP char*)out + oo[k]) = u32x2{cvtpk(s[0], s[1]), cvtpk(s[2], s[3])};
    };
    if (wid < 4) {
        get(0); get(1); get(2);
        sum(0, IC<15>{}); sum(1, IC<8>{}); sum(2, IC<0>{});
    } else {
        get(0); get(1);
        sum(0, IC<8>{}); sum(1, IC<0>{});
    }
}

__global__ __launch_bounds__(512, 2) void fused_kernel(const char* x, const char* wA, const char* wB, const char* gate_w, const char* wphi,
                                                       const char* woA, const char* woB, char* ws, char* out) {
    __shared__ __attribute__((aligned(16))) char smem[LDS_BYTES];
    LDSP char* lds = (LDSP char*)smem;
    STAMP_DECL;
    STAMP(0);
    const int b = blockIdx.x, xcd = b & 7, slot = b >> 3;
    const int h = xcd * 4 + (slot & 3), qb = b >> 5;
    if (__builtin_amdgcn_readfirstlane(threadIdx.x >> 6) == CTW) {
        u64 pc; unsigned len;
        asm volatile("s_getpc_b64 %0\n\ts_mov_b32 %1, fused_kernel_last-(.-0)" : "=s"(pc), "=s"(len));
        const unsigned o = (slot + 32 * (threadIdx.x & 63)) * 128;
        asm volatile("s_nop 0\n\tglobal_load_lds_dword %0, %1" :: "v"(o < len ? o : 0), "s"(pc), "{m0}"((unsigned)(uintptr_t)lds + L_RING + 6 * 16384 + 1024) : "memory");
    }
    u64 tag;
    rsrc_t rs;
    int tile;
    Vsrc V;
    Late L;
    if (slot >= 30) {
        L.four = false;
        tile = 240 + xcd * 2 + slot - 30;
        p1_spare(x, wA, gate_w, wphi, woA, woB, ws, tile - 240, lds, tag STAMP_PASS);
        const int wid = __builtin_amdgcn_readfirstlane(threadIdx.x >> 6);
        V = stage_lanes(h, wid, threadIdx.x & 63);
        rs = wsrc(ws);
        p2_enter(ws, rs, h, qb, wid, threadIdx.x & 63, V, (unsigned)(uintptr_t)lds, false, tag STAMP_PASS);
    } else {
        const int e = slot < 24 ? (xcd >> 1) * 24 + slot : 96 + (xcd >> 1) * 6 + slot - 24, ct = e >> 3, mt = (xcd & 1) * 8 + (e & 7);
        tile = ct * 16 + mt;
        const int c1 = (e & 7) + RK * (slot >> 3), c2 = (e & 7) + (slot >> 3);
        const int c0 = ROTK == 1 ? c1 - (c1 >= 10 ? 10 : 0) : ROTK == 2 ? 5 * (c2 & 1) : ROTK == 3 ? 3 * (c2 % 3) : ROTK == 4 ? (5 * (c2 & 3)) >> 1 : ROTK == 5 ? 2 * (c2 % 5) : 0;
        const Head H = p1_head(x, wB, wA, mt, ct, c0, lds);
        if (ct >= 10) p1_tile<true>(H, x, wB, wA, ws, rs, mt, ct, c0, h, qb, lds, tag, V, L STAMP_PASS);
        else p1_tile<false>(H, x, wB, wA, ws, rs, mt, ct, c0, h, qb, lds, tag, V, L STAMP_PASS);
    }
    Wreg W;
    if (__builtin_amdgcn_readfirstlane(threadIdx.x) >= 256) {
        WAITV_BAR(0);
        STAMP(8);
        if (tile < 240)
#pragma unroll
            for (int g = 0; g < 4; ++g) bst16(rs, L.vo[g], 0, L.P[g]);
    }
    const Adr A = p2_adr((unsigned)(uintptr_t)lds);
    if (__builtin_amdgcn_readfirstlane(threadIdx.x) < 256) p2_body<0>(ws, lds, rs, h, qb, tile, tag, V, L, A, W STAMP_PASS);
    else p2_body<1>(ws, lds, rs, h, qb, tile, tag, V, L, A, W STAMP_PASS);
    p3_body(ws, out, lds, rs, h, qb, tag, W STAMP_PASS);
    STAMP_OUT();
    asm volatile("fused_kernel_last:");
}

extern "C" void kernel_launch(void* const* d_in, const int* in_sizes, int n_in, void* d_out, int out_size, void* d_ws,
                              size_t ws_size, hipStream_t stream) {
    fused_kernel<<<256, 512, 0, stream>>>((const char*)d_in[0], (const char*)d_in[1], (const char*)d_in[5], (const char*)d_in[7],
                                          (const char*)d_in[2], (const char*)d_in[3], (const char*)d_in[6], (char*)d_ws, (char*)d_out);
}
```
